```python
import torch

def _softmax(x, axis=-1):
    return torch.softmax(x, dim=axis)
def _gelu(x):
    return torch.nn.functional.gelu(x, approximate="tanh")
def _silu(x):
    return torch.nn.functional.silu(x)
def _sigmoid(x):
    return torch.sigmoid(x)
def _layernorm(x, axis=-1, eps=1e-5):
    m = x.mean(dim=axis, keepdim=True)
    v = x.var(dim=axis, keepdim=True, unbiased=False)
    return (x - m) / torch.sqrt(v + eps)
def _scatter_add(tgt, idx, src, axis=0):
    return tgt.clone().index_add_(axis, idx, src)
def _segment_sum(src, seg, n):
    out = torch.zeros((n,) + src.shape[1:], dtype=src.dtype)
    return out.index_add_(0, seg, src)
def _pad(x, pw):
    flat = tuple(v for pair in reversed(pw) for v in pair)
    return torch.nn.functional.pad(x, flat)

kernel_name = 'recurrent_scan__sliding_block_cached__dense_activation_relu__decode'

def setup_inputs():

    _w = (lambda a: (lambda t: t.to(torch.bfloat16) if t.is_floating_point() else t)(torch.as_tensor(a)))
    def _A_setup():
        B, Sq, D = 2, 1, 1024
        Hq, Hkv, Sk, Dh = 32, 4, 32768, 128
        assert Hq % Hkv == 0, (Hq, Hkv)
        return {
            "x": _w((lambda *s: torch.randn(*s, dtype=torch.bfloat16) * 0.05)(B, Sq, D)),
            "k_cache": _w((lambda *s: torch.randn(*s, dtype=torch.bfloat16) * 1.6)(B, Hkv, Sk, Dh)),
            "v_cache": _w((lambda *s: torch.randn(*s, dtype=torch.bfloat16) * 0.2)(B, Hkv, Sk, Dh)),
            "wq": _w((lambda *s: torch.randn(*s, dtype=torch.bfloat16) * 1.6)(D, Hq * Dh) * (D ** -0.5)),
            "wo": _w((lambda *s: torch.randn(*s, dtype=torch.bfloat16) * 0.2)(Hq * Dh, D) * ((Hq * Dh) ** -0.5)),
        }
    _A_in = _A_setup()

    def _B_setup():
        B, S, D, Dff = 2, 1, 1024, 3584
        return {
            "x": _w((lambda *s: torch.randn(*s, dtype=torch.bfloat16) * 0.05)(B, S, D)),
            "w1": _w((lambda *s: torch.randn(*s, dtype=torch.bfloat16) * 0.1)(D, Dff)),
            "w2": _w((lambda *s: torch.randn(*s, dtype=torch.bfloat16) * 0.1)(Dff, D)),
        }
    _B_in = _B_setup()

    return {
        "A_x": _A_in["x"],
        "A_k_cache": _A_in["k_cache"],
        "A_v_cache": _A_in["v_cache"],
        "A_wq": _A_in["wq"],
        "A_wo": _A_in["wo"],
        "B_x": _B_in["x"],
        "B_w1": _B_in["w1"],
        "B_w2": _B_in["w2"],
    }

def reference(A_x, A_k_cache, A_v_cache, A_wq, A_wo, B_x, B_w1, B_w2):
    def _A_ref(x, k_cache, v_cache, wq, wo):
        B, Sq, D = x.shape
        Hq, Dh = 32, 128
        Hkv, Sk = k_cache.shape[1], k_cache.shape[2]
        W = min(4096, max(256, Sk // 4))
        rep = Hq // Hkv
        q = (lambda x, *d: x.permute(*d))((x @ wq).reshape(B, Sq, Hq, Dh), 0, 2, 1, 3)
        k = (lambda x, n, axis: torch.repeat_interleave(x, n, dim=axis))(k_cache[:, :, Sk - W:, :], rep, axis=1)
        v = (lambda x, n, axis: torch.repeat_interleave(x, n, dim=axis))(v_cache[:, :, Sk - W:, :], rep, axis=1)
        scores = torch.einsum("bhqd,bhkd->bhqk", q, k) * (Dh ** -0.5)
        p = _softmax(scores, axis=-1)
        o = torch.einsum("bhqk,bhkd->bhqd", p, v)
        return (lambda x, *d: x.permute(*d))(o, 0, 2, 1, 3).reshape(B, Sq, Hq * Dh) @ wo

    def _B_ref(x, w1, w2):
        h = torch.einsum("bsd,df->bsf", x, w1)
        zero = torch.zeros_like(h)
        h = torch.maximum(h, zero)
        return torch.einsum("bsf,fd->bsd", h, w2)

    x = A_x
    xn = _layernorm(x, axis=-1)
    s = _A_ref(xn, A_k_cache, A_v_cache, A_wq, A_wo)
    g = _silu(_B_ref(xn, B_w1, B_w2))
    return x + s * g
```

```cpp
#include <hip/hip_runtime.h>
#include <hip/hip_bf16.h>
#include <stdint.h>

typedef unsigned short u16;
__device__ __forceinline__ float bf2f(u16 b){ return __builtin_bit_cast(float, (unsigned)b << 16); }
__device__ __forceinline__ float rbf(float f){
  __bf16 b = (__bf16)f; return (float)b; }

constexpr int D=1024, HQ=32, HKV=4, SK=32768, DH=128, W=4096, DFF=3584;

__device__ void ln_to_lds(const u16* x, float* xn  , float* red){
  int tid=threadIdx.x;
  for(int b=0;b<2;++b){
    float v[4]; float s=0;
    for(int i=0;i<4;++i){ v[i]=bf2f(x[b*D+tid*4+i]); s+=v[i]; }
    red[tid]=s; __syncthreads();
    for(int o=128;o>0;o>>=1){ if(tid<o) red[tid]+=red[tid+o]; __syncthreads(); }
    float mean=red[0]*(1.0f/D); __syncthreads();
    float q=0; for(int i=0;i<4;++i){ float d=v[i]-mean; q+=d*d; }
    red[tid]=q; __syncthreads();
    for(int o=128;o>0;o>>=1){ if(tid<o) red[tid]+=red[tid+o]; __syncthreads(); }
    float var=rbf(red[0]*(1.0f/D)); __syncthreads();
    float mb=rbf(mean);
    float den=rbf(sqrtf(rbf(var+1e-5f)));
    for(int i=0;i<4;++i) xn[b*D+tid*4+i]=rbf(rbf(v[i]-mb)/den);
  }
  __syncthreads();
}

__global__ void k1(const u16* x, const u16* wq, const u16* w1, float* q, float* h){
  __shared__ float xn[2*D]; __shared__ float red[256]; __shared__ float part[2][4][64];
  ln_to_lds(x,xn,red);
  int tid=threadIdx.x, c=tid&63, g=tid>>6;
  int col=blockIdx.x*64+c;
  const u16* wp; int ld;
  if(col<4096){ wp=wq+col; ld=4096; } else { wp=w1+(col-4096); ld=DFF; }
  float a0=0,a1=0;
  for(int k=g*256;k<g*256+256;++k){ float w=bf2f(wp[(size_t)k*ld]); a0+=xn[k]*w; a1+=xn[D+k]*w; }
  part[0][g][c]=a0; part[1][g][c]=a1; __syncthreads();
  if(tid<128){ int b=tid>>6; float s=part[b][0][c]+part[b][1][c]+part[b][2][c]+part[b][3][c];
    s=rbf(s);
    if(col<4096) q[b*4096+col]=s; else h[b*DFF+col-4096]=fmaxf(s,0.f); }
}
__global__ void k2(const u16* kc, const u16* vc, const float* q, float* po, float* pm, float* pl){
  __shared__ float qs[8][128]; __shared__ float sc[8][128]; __shared__ float mm[8], ll[8];
  int bid=blockIdx.x, sp=bid&31, hkv=(bid>>5)&3, b=bid>>7, tid=threadIdx.x;
  for(int i=tid;i<1024;i+=256) qs[i>>7][i&127]=q[b*4096+hkv*1024+i];
  __syncthreads();
  const u16* kp=kc+((size_t)(b*HKV+hkv)*SK+(SK-W)+sp*128)*DH;
  const u16* vp=vc+((size_t)(b*HKV+hkv)*SK+(SK-W)+sp*128)*DH;
  for(int i=tid;i<1024;i+=256){ int hh=i>>7,key=i&127; float a=0; for(int d=0;d<128;++d) a+=qs[hh][d]*bf2f(kp[key*DH+d]);
    sc[hh][key]=rbf(rbf(a)*0.08838834764831845f); }
  __syncthreads();
  if(tid<8){ float m=-INFINITY; for(int k=0;k<128;++k) m=fmaxf(m,sc[tid][k]); float l=0; for(int k=0;k<128;++k){ float p=__expf(sc[tid][k]-m); l+=p; sc[tid][k]=p; } mm[tid]=m; ll[tid]=l; }
  __syncthreads();
  for(int i=tid;i<1024;i+=256){ int hh=i>>7,d=i&127; float a=0; for(int k=0;k<128;++k) a+=sc[hh][k]*bf2f(vp[k*DH+d]);
    po[(size_t)bid*1024+i]=a; }
  if(tid<8){ pm[bid*8+tid]=mm[tid]; pl[bid*8+tid]=ll[tid]; }
}
__global__ void k3(const float* po, const float* pm, const float* pl, float* o){
  int i=blockIdx.x*256+threadIdx.x;
  int b=i>>12, hq=(i>>7)&31, d=i&127, hkv=hq>>3, hh=hq&7;
  int base=(b*4+hkv)*32;
  float M=-INFINITY; for(int s=0;s<32;++s) M=fmaxf(M,pm[(base+s)*8+hh]);
  float L=0,a=0; for(int s=0;s<32;++s){ float w=__expf(pm[(base+s)*8+hh]-M); L+=w*pl[(base+s)*8+hh]; a+=w*po[(size_t)(base+s)*1024+hh*128+d]; }
  o[i]=rbf(a/L);
}
__global__ void k4(const u16* x, const u16* wo, const u16* w2, const float* o, const float* h, __hip_bfloat16* out){
  __shared__ float part[4][16][16];
  int tid=threadIdx.x, c=tid&15, g=tid>>4; int col=blockIdx.x*16+c;
  float s0=0,s1=0,g0=0,g1=0;
  for(int k=g;k<4096;k+=16){ float w=bf2f(wo[(size_t)k*D+col]); s0+=o[k]*w; s1+=o[4096+k]*w; }
  for(int k=g;k<DFF;k+=16){ float w=bf2f(w2[(size_t)k*D+col]); g0+=h[k]*w; g1+=h[DFF+k]*w; }
  part[0][g][c]=s0; part[1][g][c]=s1; part[2][g][c]=g0; part[3][g][c]=g1; __syncthreads();
  if(tid<32){ int b=tid>>4; float s=0,gg=0; for(int j=0;j<16;++j){ s+=part[b][j][c]; gg+=part[2+b][j][c]; }
    s=rbf(s); gg=rbf(gg); float sl=rbf(gg/(1.0f+__expf(-gg))); float r=rbf(s*sl); float xo=bf2f(x[b*D+col]);
    out[b*D+col]=__float2bfloat16(xo+r); }
}

extern "C" void kernel_launch(void* const* d_in, const int* in_sizes, int n_in, void* d_out, int out_size, void* d_ws, size_t ws_size, hipStream_t stream) {
  const u16* x=(const u16*)d_in[0]; const u16* kc=(const u16*)d_in[1]; const u16* vc=(const u16*)d_in[2];
  const u16* wq=(const u16*)d_in[3]; const u16* wo=(const u16*)d_in[4]; const u16* w1=(const u16*)d_in[6]; const u16* w2=(const u16*)d_in[7];
  float* ws=(float*)d_ws; float* q=ws; float* h=ws+8192; float* o=ws+16384; float* pm=ws+32768; float* pl=ws+36864; float* po=ws+65536;
  k1<<<120,256,0,stream>>>(x,wq,w1,q,h);
  k2<<<256,256,0,stream>>>(kc,vc,q,po,pm,pl);
  k3<<<32,256,0,stream>>>(po,pm,pl,o);
  k4<<<64,256,0,stream>>>(x,wo,w2,o,h,(__hip_bfloat16*)d_out);
}
```

```cpp
#include <hip/hip_runtime.h>
#include <hip/hip_bf16.h>
#include <stdint.h>

typedef unsigned int u32;
typedef unsigned long long u64;
typedef u32 u32x4 __attribute__((ext_vector_type(4)));
typedef u32 u32x2 __attribute__((ext_vector_type(2)));
typedef float f32x4 __attribute__((ext_vector_type(4)));
typedef float f32x2 __attribute__((ext_vector_type(2)));
typedef short bf16x8 __attribute__((ext_vector_type(8)));
typedef __amdgpu_buffer_rsrc_t rsrc_t;
#define DI __device__ __forceinline__
#define SB() __builtin_amdgcn_sched_barrier(0)

constexpr u32 EP_OFF = 0;
constexpr u32 QP_OFF = 4096;
constexpr u32 HP_OFF = QP_OFF + 32 * 8 * 2 * 128 * 8;
constexpr u32 PO_OFF = HP_OFF + 28 * 8 * 2 * 128 * 8;
constexpr u32 ST_OFF = PO_OFF + 256 * 1024 * 8;
constexpr u32 SP_OFF = ST_OFF + 256 * 16 * 8;
constexpr u32 GP_OFF = SP_OFF + 64 * 2048 * 8;
constexpr u32 WS_USED = GP_OFF + 28 * 2048 * 8;
constexpr u32 OOB = 0xFFFFFF00u;
constexpr u32 SPIN_CAP = 1u << 16;
#define STAMP(i) do { } while (0)

constexpr int L_X = 0;
constexpr int RS = 576;
constexpr int RPL = 8 * RS;
constexpr int L_RQ = 4096;
constexpr int L_RH = L_RQ + 2 * RPL;
constexpr int L_RS = L_RH + 2 * RPL;
constexpr int L_RG = L_RS + 2 * RPL;
constexpr int L_Q = L_RG + 2 * RPL;
constexpr int QS = 272;
constexpr int L_ML = L_Q + 8 * QS;
constexpr int L_PART = L_ML + 512;
constexpr int L_O = L_PART + 2048;
constexpr int L_END = L_O + 8 * 4096;
constexpr int LDS_BYTES = 84 * 1024;
static_assert(L_END <= LDS_BYTES, "lds");

DI rsrc_t mk(const void* p, u32 bytes) { return __builtin_amdgcn_make_buffer_rsrc((void*)p, 0, bytes, 0x00020000); }
DI u32x4 ldw(rsrc_t r, u32 off) { return __builtin_amdgcn_raw_buffer_load_b128(r, off, 0, 2); }
DI u32x4 ldc(rsrc_t r, u32 off) { return __builtin_amdgcn_raw_buffer_load_b128(r, off, 0, 16); }
DI void stc(rsrc_t r, u32 off, u32x4 v) { __builtin_amdgcn_raw_buffer_store_b128(v, r, off, 0, 16); }
DI float asf(u32 u) { return __builtin_bit_cast(float, u); }
DI u32 asu(float f) { return __builtin_bit_cast(u32, f); }
DI float rbf(float f) { return (float)(__bf16)f; }
DI float blo(u32 u) { return asf(u << 16); }
DI float bhi(u32 u) { return asf(u & 0xFFFF0000u); }
DI f32x4 mfma(u32x4 a, u32x4 b, f32x4 c) {
  return __builtin_amdgcn_mfma_f32_16x16x32_bf16(__builtin_bit_cast(bf16x8, a), __builtin_bit_cast(bf16x8, b), c, 0, 0, 0);
}
template <int CTRL> DI float dpp(float x) {
  return __builtin_bit_cast(float, __builtin_amdgcn_mov_dpp(__builtin_bit_cast(int, x), CTRL, 0xf, 0xf, true));
}
constexpr int XOR1 = 0xB1, XOR2 = 0x4E, ROR8 = 0x128, ROR4 = 0x124;
DI float sum16(float x) { auto s = __builtin_amdgcn_permlane16_swap(asu(x), asu(x), false, false); return asf(s[0]) + asf(s[1]); }
DI float sum32(float x) { auto s = __builtin_amdgcn_permlane32_swap(asu(x), asu(x), false, false); return asf(s[0]) + asf(s[1]); }
DI float max16(float x) { auto s = __builtin_amdgcn_permlane16_swap(asu(x), asu(x), false, false); return fmaxf(asf(s[0]), asf(s[1])); }
DI float max32(float x) { auto s = __builtin_amdgcn_permlane32_swap(asu(x), asu(x), false, false); return fmaxf(asf(s[0]), asf(s[1])); }
DI float rowsum(float x) { x += dpp<XOR1>(x); x += dpp<XOR2>(x); x += dpp<ROR4>(x); x += dpp<ROR8>(x); return x; }
DI float rowmax(float x) { x = fmaxf(x, dpp<XOR1>(x)); x = fmaxf(x, dpp<XOR2>(x)); x = fmaxf(x, dpp<ROR4>(x)); x = fmaxf(x, dpp<ROR8>(x)); return x; }
DI void lbar() { asm volatile("s_waitcnt lgkmcnt(0)" ::: "memory"); __builtin_amdgcn_s_barrier(); asm volatile("" ::: "memory"); }
DI void pin(u32x4 v) { asm volatile("" ::"v"(v)); }

struct Tag { u32 hi, lo; };
DI u32x4 pack2(float a, float b, Tag t) {
  u32x4 v;
  v[0] = ((asu(a) + 0x80u) & 0xFFFFFF00u) | t.lo; v[1] = t.hi;
  v[2] = ((asu(b) + 0x80u) & 0xFFFFFF00u) | t.lo; v[3] = t.hi;
  return v;
}
DI u32 bad2(u32x4 v, Tag t) { return (v[1] ^ t.hi) | (v[3] ^ t.hi) | (((v[0] ^ t.lo) | (v[2] ^ t.lo)) & 0xFFu); }
DI float pay(u32 u) { return asf(u & 0xFFFFFF00u); }
DI bool any_bad(u32 bad) { return __builtin_amdgcn_ballot_w64(bad != 0) != 0; }

DI u32x4 onehot(u32 dup, const u32 (&mk4)[4]) { u32x4 a; a[0] = dup & mk4[0]; a[1] = dup & mk4[1]; a[2] = dup & mk4[2]; a[3] = dup & mk4[3]; return a; }
DI u32 dupbf(float f) { u32 u = asu(rbf(f)); return u | (u >> 16); }

extern __shared__ __attribute__((aligned(16))) char smem[];
template <class T> DI T lget(int off) { return *(const T*)(smem + off); }
template <class T> DI void lput(int off, T v) { *(T*)(smem + off) = v; }

DI void red_write(int base, int w, int g, int n, f32x4 acc) {
  const int o = base + w * RS + (4 * n + g) * 8;
  lput<f32x2>(o, f32x2{acc[0], acc[1]});
  lput<f32x2>(o + RPL, f32x2{acc[2], acc[3]});
}

__global__ __launch_bounds__(512, 2) void fused_kernel(const void* __restrict__ x, const void* __restrict__ kc, const void* __restrict__ vc,
                                                       const void* __restrict__ wq, const void* __restrict__ wo, const void* __restrict__ w1,
                                                       const void* __restrict__ w2, u32* __restrict__ out, void* ws) {
  STAMP(0);
  asm volatile("" ::"s"(x), "s"(kc), "s"(vc), "s"(wq), "s"(wo), "s"(w1), "s"(w2), "s"(out), "s"(ws));
  const int bid = blockIdx.x;
  const int xcd = bid & 7, jj = bid >> 3, p = xcd * 32 + jj;
  const int tid = threadIdx.x, lane = tid & 63, g = lane >> 4, n = lane & 15;
  const int w = __builtin_amdgcn_readfirstlane(tid >> 6);

  const u32x2 xr = __builtin_amdgcn_raw_buffer_load_b64(mk(x, 4096), tid * 8, 0, 0);
  const int trow = xcd * 128 + w * 16 + g * 4;
  u32x4 WQ[4], K[4], V[4], W1[4], WO[4], W2[4];
  {
    const rsrc_t r = mk(wq, 1024 * 4096 * 2);
#pragma unroll
    for (int j = 0; j < 4; ++j) WQ[j] = ldw(r, (trow + j) * 8192 + jj * 256 + n * 16);
  }
  SB();
  const u32 kvbase = ((u32)(((xcd & 1) * 4 + (xcd >> 1)) * 32768 + 28672 + jj * 128 + w * 16)) * 256u;
  {
    const rsrc_t r = mk(kc, 2u * 4 * 32768 * 128 * 2);
#pragma unroll
    for (int t = 0; t < 4; ++t) K[t] = ldw(r, kvbase + n * 256 + t * 64 + g * 16);
  }
  SB();
  STAMP(1);
  const rsrc_t rw = mk(ws, WS_USED);
  Tag tag;
  u64 newep;
  {
    const u64 e = *(const __attribute__((address_space(4))) u64*)(uintptr_t)ws;
    const u64 t40 = ((e >> 24) * 6364136223846793005ull + 1442695040888963407ull) & 0xFFFFFFFFFFull;
    tag.hi = (u32)(t40 >> 8);
    tag.lo = (u32)(t40 & 0xFF);
    newep = t40 << 24;
  }
  u32 mk4[4];
#pragma unroll
  for (int i = 0; i < 4; ++i) mk4[i] = (i == (n >> 2)) ? ((n & 1) ? 0xFFFF0000u : 0x0000FFFFu) : 0u;
  const int mb = (n >> 1) & 1;

  lput<u32x2>(L_X + tid * 8, xr);
  lbar();
  u32x4 AX[4];
  {
    float v[32];
    const int hb = lane >> 5;
#pragma unroll
    for (int i = 0; i < 4; ++i) {
      const u32x4 t = lget<u32x4>(L_X + hb * 2048 + i * 512 + (lane & 31) * 16);
#pragma unroll
      for (int k = 0; k < 4; ++k) { v[i * 8 + 2 * k] = blo(t[k]); v[i * 8 + 2 * k + 1] = bhi(t[k]); }
    }
    const u32x2 xa = lget<u32x2>(L_X + mb * 2048 + trow * 2);
    float s0 = 0, s1 = 0, s2 = 0, s3 = 0;
#pragma unroll
    for (int i = 0; i < 32; i += 4) { s0 += v[i]; s1 += v[i + 1]; s2 += v[i + 2]; s3 += v[i + 3]; }
    const float mean = sum16(rowsum((s0 + s1) + (s2 + s3))) * (1.0f / 1024.0f);
    s0 = s1 = s2 = s3 = 0;
#pragma unroll
    for (int i = 0; i < 32; i += 4) {
      float d0 = v[i] - mean, d1 = v[i + 1] - mean, d2 = v[i + 2] - mean, d3 = v[i + 3] - mean;
      s0 += d0 * d0; s1 += d1 * d1; s2 += d2 * d2; s3 += d3 * d3;
    }
    const float var = sum16(rowsum((s0 + s1) + (s2 + s3))) * (1.0f / 1024.0f);
    const float mr = rbf(mean), den = rbf(__builtin_sqrtf(rbf(rbf(var) + 1e-5f)));
    const float m0 = asf(__builtin_amdgcn_readlane(asu(mr), 0)), m1 = asf(__builtin_amdgcn_readlane(asu(mr), 32));
    const float d0 = asf(__builtin_amdgcn_readlane(asu(den), 0)), d1 = asf(__builtin_amdgcn_readlane(asu(den), 32));
    const float mm = mb ? m1 : m0, dd = mb ? d1 : d0;
    AX[0] = onehot(dupbf(rbf(blo(xa[0]) - mm) / dd), mk4);
    AX[1] = onehot(dupbf(rbf(bhi(xa[0]) - mm) / dd), mk4);
    AX[2] = onehot(dupbf(rbf(blo(xa[1]) - mm) / dd), mk4);
    AX[3] = onehot(dupbf(rbf(bhi(xa[1]) - mm) / dd), mk4);
  }
  SB();
  STAMP(2);
  {
    const rsrc_t r = mk(vc, 2u * 4 * 32768 * 128 * 2);
    f32x4 acc = {0, 0, 0, 0};
#pragma unroll
    for (int j = 0; j < 4; ++j) {
      acc = mfma(AX[j], WQ[j], acc);
      SB();
      V[j] = ldw(r, kvbase + (g * 4 + j) * 256 + n * 16);
      SB();
    }
    STAMP(3);
    red_write(L_RQ, w, g, n, acc);
  }
  lbar();
  const int sub4 = lane & 3, pi = lane >> 2;
  {
    const int o = L_RQ + (w >> 2) * RPL + sub4 * 2 * RS + ((w & 3) * 16 + pi) * 8;
    const f32x2 a = lget<f32x2>(o), b = lget<f32x2>(o + RS);
    float u0 = a[0] + b[0], u1 = a[1] + b[1];
    u0 += dpp<XOR1>(u0); u1 += dpp<XOR1>(u1);
    u0 += dpp<XOR2>(u0); u1 += dpp<XOR2>(u1);
    stc(rw, sub4 == 0 ? QP_OFF + (jj * 8 + xcd) * 2048 + (w * 16 + pi) * 16 : OOB, pack2(u0, u1, tag));
  }
  SB();
  STAMP(4);
  pin(K[3]);
  SB();
  STAMP(5);
  const int hkv = xcd >> 1;
  const int cb1 = (jj + 1) & 31;
  const bool has1 = cb1 < 28;
  {
    u32x4 Q[8];
    const u32 qo = QP_OFF + ((hkv * 8 + w) * 8 * 2 + (xcd & 1)) * 1024 + lane * 16;
#pragma unroll
    for (int i = 0; i < 8; ++i) Q[i] = ldc(rw, qo + i * 2048);
    SB();
    {
      const rsrc_t r = mk(w1, has1 ? 1024 * 3584 * 2 : 0);
#pragma unroll
      for (int j = 0; j < 4; ++j) W1[j] = ldw(r, (trow + j) * 7168 + cb1 * 256 + n * 16);
    }
    SB();
    u32 bad = 0;
#pragma unroll
    for (int i = 0; i < 8; ++i) bad |= bad2(Q[i], tag);
    STAMP(6);
    for (u32 spin = 0; any_bad(bad) && spin < SPIN_CAP; ++spin) {
      asm volatile("" ::: "memory");
      bad = 0;
#pragma unroll
      for (int i = 0; i < 8; ++i) Q[i] = ldc(rw, qo + i * 2048);
#pragma unroll
      for (int i = 0; i < 8; ++i) bad |= bad2(Q[i], tag);
    }
    float q0 = 0, q1 = 0;
#pragma unroll
    for (int i = 0; i < 8; ++i) { q0 += pay(Q[i][0]); q1 += pay(Q[i][2]); }
    lput<u32>(L_Q + w * QS + lane * 4, (asu(rbf(q0)) >> 16) | asu(rbf(q1)));
  }
  lbar();
  STAMP(7);
  {
    f32x4 s = {0, 0, 0, 0};
#pragma unroll
    for (int t = 0; t < 4; ++t) s = mfma(K[t], lget<u32x4>(L_Q + (n & 7) * QS + t * 64 + g * 16), s);
    float sc[4];
#pragma unroll
    for (int r = 0; r < 4; ++r) sc[r] = rbf(rbf(s[r]) * 0.08838834764831845f);
    const float m = max32(max16(fmaxf(fmaxf(sc[0], sc[1]), fmaxf(sc[2], sc[3]))));
    u32x4 P;
    float l = 0;
    const u32 sh = (n & 8) ? 0u : 16u;
#pragma unroll
    for (int r = 0; r < 4; ++r) { const float e = __expf(sc[r] - m); l += e; P[r] = asu(rbf(e)) >> sh; }
    l = sum32(sum16(l));
    if (lane < 8) lput<f32x2>(L_ML + (w * 8 + lane) * 8, f32x2{m, l});
#pragma unroll
    for (int i = 0; i < 4; ++i) {
      const u32x4 B = {V[0][i], V[1][i], V[2][i], V[3][i]};
      const f32x4 o = mfma(P, B, f32x4{0, 0, 0, 0});
      lput<f32x4>(L_O + w * 4096 + (g & 1) * 2048 + (i * 32 + (g >> 1) * 16 + n) * 16, o);
    }
  }
  lbar();
  {
    const f32x2 ml = lget<f32x2>(L_ML + lane * 8);
    f32x2 ov[8];
    const int a = w >> 1;
#pragma unroll
    for (int i = 0; i < 8; ++i) ov[i] = lget<f32x2>(L_O + i * 4096 + (a >> 1) * 2048 + ((w & 1) * 64 + lane) * 16 + (a & 1) * 8);
    const float M = max32(max16(fmaxf(ml[0], dpp<ROR8>(ml[0]))));
    const float sc = __expf(ml[0] - M);
    float L = sc * ml[1];
    L += dpp<ROR8>(L);
    L = sum32(sum16(L));
    float o0 = 0, o1 = 0;
#pragma unroll
    for (int i = 0; i < 8; ++i) {
      o0 += asf(__builtin_amdgcn_readlane(asu(sc), i * 8 + 2 * a)) * ov[i][0];
      o1 += asf(__builtin_amdgcn_readlane(asu(sc), i * 8 + 2 * a + 1)) * ov[i][1];
    }
    stc(rw, PO_OFF + p * 8192 + w * 1024 + lane * 16, pack2(o0, o1, tag));
    stc(rw, (tid < 8) ? ST_OFF + p * 128 + lane * 16 : OOB, pack2(M, L, tag));
  }
  SB();
  STAMP(8);
  const int ks = p >> 2, ca = (ks >> 2) & 3, cc = ks & 3;
  {
    const rsrc_t r = mk(wo, 4096 * 1024 * 2);
    const int rg = w & 3;
    const int row0 = (hkv * 8 + 2 * ca + (rg >> 1)) * 128 + 32 * g + 2 * cc + (rg & 1);
#pragma unroll
    for (int j = 0; j < 4; ++j) WO[j] = ldw(r, (row0 + 8 * j) * 2048 + (p & 3) * 512 + (w >> 2) * 256 + n * 16);
  }
  SB();
  {
    f32x4 acc = {0, 0, 0, 0};
#pragma unroll
    for (int j = 0; j < 4; ++j) acc = mfma(AX[j], W1[j], acc);
    red_write(L_RH, w, g, n, acc);
  }
  STAMP(9);
  lbar();
  {
    const int o = L_RH + (w >> 2) * RPL + sub4 * 2 * RS + ((w & 3) * 16 + pi) * 8;
    const f32x2 a = lget<f32x2>(o), b = lget<f32x2>(o + RS);
    float u0 = a[0] + b[0], u1 = a[1] + b[1];
    u0 += dpp<XOR1>(u0); u1 += dpp<XOR1>(u1);
    u0 += dpp<XOR2>(u0); u1 += dpp<XOR2>(u1);
    stc(rw, (sub4 == 0 && has1) ? HP_OFF + (cb1 * 8 + xcd) * 2048 + (w * 16 + pi) * 16 : OOB, pack2(u0, u1, tag));
  }
  SB();
  const int j2 = (jj + 14) & 31;
  const bool has2 = j2 < 28;
  const int t2 = xcd * 28 + j2, k2 = t2 >> 3, cb2 = t2 & 7;
  u32x4 H[2];
  const u32 ho = has2 ? HP_OFF + ((k2 * 8 + 2 * (n >> 2) + (n & 1)) * 2 + mb) * 1024 + (w * 16 + g * 4) * 8 : OOB;
  {
    u32x4 PO[4], ST;
    const int pb = hkv * 64 + (w >> 2) * 32;
    const int grp = 2 * (w & 3) + (lane >> 5);
    const u32 po = PO_OFF + (pb + grp * 4) * 8192 + ca * 2048 + cc * 512 + (lane & 31) * 16;
    const u32 so = ST_OFF + (pb + (lane & 31)) * 128 + (2 * ca + (lane >> 5)) * 16;
    ST = ldc(rw, so);
#pragma unroll
    for (int i = 0; i < 4; ++i) PO[i] = ldc(rw, po + i * 8192);
    SB();
    {
      const rsrc_t r = mk(w2, has2 ? 3584 * 1024 * 2 : 0);
#pragma unroll
      for (int j = 0; j < 4; ++j) W2[j] = ldw(r, (k2 * 128 + w * 16 + g * 4 + j) * 2048 + cb2 * 256 + n * 16);
    }
    SB();
    u32 bad = bad2(ST, tag);
#pragma unroll
    for (int i = 0; i < 4; ++i) bad |= bad2(PO[i], tag);
    STAMP(10);
    for (u32 spin = 0; any_bad(bad) && spin < SPIN_CAP; ++spin) {
      asm volatile("" ::: "memory");
      ST = ldc(rw, so);
#pragma unroll
      for (int i = 0; i < 4; ++i) PO[i] = ldc(rw, po + i * 8192);
      bad = bad2(ST, tag);
#pragma unroll
      for (int i = 0; i < 4; ++i) bad |= bad2(PO[i], tag);
    }
    SB();
    H[0] = ldc(rw, ho);
    H[1] = ldc(rw, ho + 16);
    SB();
    const float m = pay(ST[0]);
    const float M = max16(rowmax(m));
    const float e = __expf(m - M);
    const float L = sum16(rowsum(e * pay(ST[2])));
    const u32 wn = asu(e / L);
    float o0 = 0, o1 = 0;
#pragma unroll
    for (int i = 0; i < 4; ++i) {
      o0 += asf(__builtin_amdgcn_ds_bpermute((grp * 4 + i) * 4, wn)) * pay(PO[i][0]);
      o1 += asf(__builtin_amdgcn_ds_bpermute((32 + grp * 4 + i) * 4, wn)) * pay(PO[i][2]);
    }
    auto sw = __builtin_amdgcn_permlane32_swap(asu(o0), asu(o1), false, false);
    lput<float>(L_PART + (w & 3) * 512 + (w >> 2) * 256 + lane * 4, asf(sw[0]) + asf(sw[1]));
  }
  lbar();
  STAMP(11);
  {
    f32x4 o = lget<f32x4>(L_PART + mb * 256 + ((w & 3) * 16 + g * 4) * 4);
#pragma unroll
    for (int i = 1; i < 4; ++i) o += lget<f32x4>(L_PART + i * 512 + mb * 256 + ((w & 3) * 16 + g * 4) * 4);
    f32x4 acc = {0, 0, 0, 0};
#pragma unroll
    for (int j = 0; j < 4; ++j) acc = mfma(onehot(dupbf(o[j]), mk4), WO[j], acc);
    red_write(L_RS, w, g, n, acc);
  }
  lbar();
  {
    const int P = tid >> 1, sub = lane & 1;
    const int ch = P >> 7, nn = (P >> 3) & 15, bb = (P >> 2) & 1, gg = P & 3;
    const int o = L_RS + bb * RPL + (ch * 4 + sub * 2) * RS + (nn * 4 + gg) * 8;
    const f32x2 a = lget<f32x2>(o), b = lget<f32x2>(o + RS);
    float u0 = a[0] + b[0], u1 = a[1] + b[1];
    u0 += dpp<XOR1>(u0); u1 += dpp<XOR1>(u1);
    stc(rw, sub == 0 ? SP_OFF + ks * 16384 + (p & 3) * 4096 + P * 16 : OOB, pack2(u0, u1, tag));
  }
  SB();
  STAMP(12);
  {
    u32 bad = has2 ? (bad2(H[0], tag) | bad2(H[1], tag)) : 0u;
    for (u32 spin = 0; any_bad(bad) && spin < SPIN_CAP; ++spin) {
      asm volatile("" ::: "memory");
      H[0] = ldc(rw, ho);
      H[1] = ldc(rw, ho + 16);
      bad = bad2(H[0], tag) | bad2(H[1], tag);
    }
    float h[4] = {pay(H[0][0]), pay(H[0][2]), pay(H[1][0]), pay(H[1][2])};
    f32x4 acc = {0, 0, 0, 0};
#pragma unroll
    for (int j = 0; j < 4; ++j) {
      float t = h[j];
      t += dpp<XOR1>(t); t += dpp<ROR4>(t); t += dpp<ROR8>(t);
      acc = mfma(onehot(dupbf(fmaxf(t, 0.0f)), mk4), W2[j], acc);
    }
    red_write(L_RG, w, g, n, acc);
  }
  STAMP(13);
  lbar();
  {
    const int P = w * 16 + pi;
    const int nn = P >> 3, bb = (P >> 2) & 1, gg = P & 3;
    const int o = L_RG + bb * RPL + sub4 * 2 * RS + (nn * 4 + gg) * 8;
    const f32x2 a = lget<f32x2>(o), b = lget<f32x2>(o + RS);
    float u0 = a[0] + b[0], u1 = a[1] + b[1];
    u0 += dpp<XOR1>(u0); u1 += dpp<XOR1>(u1);
    u0 += dpp<XOR2>(u0); u1 += dpp<XOR2>(u1);
    stc(rw, (sub4 == 0 && has2) ? GP_OFF + k2 * 16384 + cb2 * 2048 + P * 16 : OOB, pack2(u0, u1, tag));
  }
  SB();
  STAMP(14);
  if (w == 7 && (p & 1) == 0) {
    const int f = p >> 1, kg = lane >> 3, u = lane & 7;
    const u32 so = SP_OFF + kg * 16384 + f * 128 + u * 16;
    const u32 go = GP_OFF + kg * 16384 + f * 128 + u * 16;
    u32x4 S[8], G[4];
    u32 bad = 1;
    for (u32 spin = 0; any_bad(bad) && spin < SPIN_CAP; ++spin) {
      asm volatile("" ::: "memory");
#pragma unroll
      for (int i = 0; i < 8; ++i) S[i] = ldc(rw, so + i * 8 * 16384);
      bad = 0;
#pragma unroll
      for (int i = 0; i < 8; ++i) bad |= bad2(S[i], tag);
    }
    float s0 = 0, s1 = 0;
#pragma unroll
    for (int i = 0; i < 8; ++i) { s0 += pay(S[i][0]); s1 += pay(S[i][2]); }
    bad = 1;
    for (u32 spin = 0; any_bad(bad) && spin < SPIN_CAP; ++spin) {
      asm volatile("" ::: "memory");
#pragma unroll
      for (int i = 0; i < 4; ++i) G[i] = ldc(rw, (i < 3 || kg < 4) ? go + i * 8 * 16384 : OOB);
      bad = bad2(G[0], tag) | bad2(G[1], tag) | bad2(G[2], tag) | (kg < 4 ? bad2(G[3], tag) : 0u);
    }
    float g0 = 0, g1 = 0;
#pragma unroll
    for (int i = 0; i < 4; ++i) { g0 += pay(G[i][0]); g1 += pay(G[i][2]); }
    s0 += dpp<ROR8>(s0); s1 += dpp<ROR8>(s1); g0 += dpp<ROR8>(g0); g1 += dpp<ROR8>(g1);
    s0 = rbf(sum32(sum16(s0))); s1 = rbf(sum32(sum16(s1)));
    g0 = rbf(sum32(sum16(g0))); g1 = rbf(sum32(sum16(g1)));
    const int idx = (u >> 2) * 512 + f * 4 + (u & 3);
    const u32 xv = lget<u32>(L_X + idx * 4);
    const float r0 = rbf(s0 * rbf(g0 / (1.0f + __expf(-g0)))), r1 = rbf(s1 * rbf(g1 / (1.0f + __expf(-g1))));
    if (lane < 8) out[idx] = (asu(rbf(blo(xv) + r0)) >> 16) | asu(rbf(bhi(xv) + r1));
    if (p == 0 && lane == 0) __hip_atomic_store((u64*)ws, newep, __ATOMIC_RELAXED, __HIP_MEMORY_SCOPE_AGENT);
  }
}

extern "C" void kernel_launch(void* const* d_in, const int* in_sizes, int n_in, void* d_out, int out_size, void* d_ws,
                              size_t ws_size, hipStream_t stream) {
  (void)in_sizes; (void)n_in; (void)out_size; (void)ws_size;
  (void)hipFuncSetAttribute((const void*)fused_kernel, hipFuncAttributeMaxDynamicSharedMemorySize, LDS_BYTES);
  fused_kernel<<<256, 512, LDS_BYTES, stream>>>(d_in[0], d_in[1], d_in[2], d_in[3], d_in[4], d_in[6], d_in[7], (u32*)d_out, d_ws);
}
```

```cpp
#include <hip/hip_runtime.h>
#include <hip/hip_bf16.h>
#include <stdint.h>

typedef unsigned int u32;
typedef unsigned long long u64;
typedef u32 u32x4 __attribute__((ext_vector_type(4)));
typedef u32 u32x2 __attribute__((ext_vector_type(2)));
typedef float f32x4 __attribute__((ext_vector_type(4)));
typedef float f32x2 __attribute__((ext_vector_type(2)));
typedef short bf16x8 __attribute__((ext_vector_type(8)));
typedef __amdgpu_buffer_rsrc_t rsrc_t;
#define DI __device__ __forceinline__
#define SB() __builtin_amdgcn_sched_barrier(0)

constexpr u32 EP_OFF = 0;
constexpr u32 QP_OFF = 4096;
constexpr u32 HP_OFF = QP_OFF + 32 * 8 * 2 * 128 * 8;
constexpr u32 PO_OFF = HP_OFF + 28 * 8 * 2 * 128 * 8;
constexpr u32 ST_OFF = PO_OFF + 256 * 1024 * 8;
constexpr u32 SP_OFF = ST_OFF + 256 * 16 * 8;
constexpr u32 GP_OFF = SP_OFF + 64 * 2048 * 8;
constexpr u32 WS_USED = GP_OFF + 28 * 2048 * 8;
constexpr u32 OOB = 0xFFFFFF00u;
constexpr u32 SPIN_CAP = 1u << 16;
#define STAMP(i) do { } while (0)

constexpr int L_X = 0;
constexpr int RS = 576;
constexpr int RPL = 8 * RS;
constexpr int L_RQ = 4096;
constexpr int L_RH = L_RQ + 2 * RPL;
constexpr int L_RS = L_RH + 2 * RPL;
constexpr int L_RG = L_RS + 2 * RPL;
constexpr int L_Q = L_RG + 2 * RPL;
constexpr int QS = 272;
constexpr int L_ML = L_Q + 8 * QS;
constexpr int L_PART = L_ML + 512;
constexpr int L_O = L_PART + 2048;
constexpr int L_END = L_O + 8 * 4096;
constexpr int LDS_BYTES = 84 * 1024;
static_assert(L_END <= LDS_BYTES, "lds");

DI rsrc_t mk(const void* p, u32 bytes) { return __builtin_amdgcn_make_buffer_rsrc((void*)p, 0, bytes, 0x00020000); }
DI u32x4 ldw(rsrc_t r, u32 off) { return __builtin_amdgcn_raw_buffer_load_b128(r, off, 0, 2); }
DI u32x4 ldc(rsrc_t r, u32 off) { return __builtin_amdgcn_raw_buffer_load_b128(r, off, 0, 16); }
DI void stc(rsrc_t r, u32 off, u32x4 v) { __builtin_amdgcn_raw_buffer_store_b128(v, r, off, 0, 16); }
DI float asf(u32 u) { return __builtin_bit_cast(float, u); }
DI u32 asu(float f) { return __builtin_bit_cast(u32, f); }
DI float rbf(float f) { return (float)(__bf16)f; }
DI float blo(u32 u) { return asf(u << 16); }
DI float bhi(u32 u) { return asf(u & 0xFFFF0000u); }
DI f32x4 mfma(u32x4 a, u32x4 b, f32x4 c) {
  return __builtin_amdgcn_mfma_f32_16x16x32_bf16(__builtin_bit_cast(bf16x8, a), __builtin_bit_cast(bf16x8, b), c, 0, 0, 0);
}
template <int CTRL> DI float dpp(float x) {
  return __builtin_bit_cast(float, __builtin_amdgcn_mov_dpp(__builtin_bit_cast(int, x), CTRL, 0xf, 0xf, true));
}
constexpr int XOR1 = 0xB1, XOR2 = 0x4E, ROR8 = 0x128, ROR4 = 0x124;
DI float sum16(float x) { auto s = __builtin_amdgcn_permlane16_swap(asu(x), asu(x), false, false); return asf(s[0]) + asf(s[1]); }
DI float sum32(float x) { auto s = __builtin_amdgcn_permlane32_swap(asu(x), asu(x), false, false); return asf(s[0]) + asf(s[1]); }
DI float max16(float x) { auto s = __builtin_amdgcn_permlane16_swap(asu(x), asu(x), false, false); return fmaxf(asf(s[0]), asf(s[1])); }
DI float max32(float x) { auto s = __builtin_amdgcn_permlane32_swap(asu(x), asu(x), false, false); return fmaxf(asf(s[0]), asf(s[1])); }
DI float rowsum(float x) { x += dpp<XOR1>(x); x += dpp<XOR2>(x); x += dpp<ROR4>(x); x += dpp<ROR8>(x); return x; }
DI float rowmax(float x) { x = fmaxf(x, dpp<XOR1>(x)); x = fmaxf(x, dpp<XOR2>(x)); x = fmaxf(x, dpp<ROR4>(x)); x = fmaxf(x, dpp<ROR8>(x)); return x; }
DI void lbar() { asm volatile("s_waitcnt lgkmcnt(0)" ::: "memory"); __builtin_amdgcn_s_barrier(); asm volatile("" ::: "memory"); }
DI void pin(u32x4 v) { asm volatile("" ::"v"(v)); }

struct Tag { u32 hi, lo; };
DI u32x4 pack2(float a, float b, Tag t) {
  u32x4 v;
  v[0] = ((asu(a) + 0x80u) & 0xFFFFFF00u) | t.lo; v[1] = t.hi;
  v[2] = ((asu(b) + 0x80u) & 0xFFFFFF00u) | t.lo; v[3] = t.hi;
  return v;
}
DI u32 bad2(u32x4 v, Tag t) { return (v[1] ^ t.hi) | (v[3] ^ t.hi) | (((v[0] ^ t.lo) | (v[2] ^ t.lo)) & 0xFFu); }
DI float pay(u32 u) { return asf(u & 0xFFFFFF00u); }
DI bool any_bad(u32 bad) { return __builtin_amdgcn_ballot_w64(bad != 0) != 0; }

DI u32x4 onehot(u32 dup, const u32 (&mk4)[4]) { u32x4 a; a[0] = dup & mk4[0]; a[1] = dup & mk4[1]; a[2] = dup & mk4[2]; a[3] = dup & mk4[3]; return a; }
DI u32 dupbf(float f) { u32 u = asu(rbf(f)); return u | (u >> 16); }

extern __shared__ __attribute__((aligned(16))) char smem[];
template <class T> DI T lget(int off) { return *(const T*)(smem + off); }
template <class T> DI void lput(int off, T v) { *(T*)(smem + off) = v; }

DI void red_write(int base, int w, int g, int n, f32x4 acc) {
  const int o = base + w * RS + (4 * n + g) * 8;
  lput<f32x2>(o, f32x2{acc[0], acc[1]});
  lput<f32x2>(o + RPL, f32x2{acc[2], acc[3]});
}

__global__ __launch_bounds__(512, 2) void fused_kernel(const void* __restrict__ x, const void* __restrict__ kc, const void* __restrict__ vc,
                                                       const void* __restrict__ wq, const void* __restrict__ wo, const void* __restrict__ w1,
                                                       const void* __restrict__ w2, u32* __restrict__ out, void* ws) {
  STAMP(0);
  asm volatile("" ::"s"(x), "s"(kc), "s"(vc), "s"(wq), "s"(wo), "s"(w1), "s"(w2), "s"(out), "s"(ws));
  const int bid = blockIdx.x;
  const int xcd = bid & 7, jj = bid >> 3, p = xcd * 32 + jj;
  const int tid = threadIdx.x, lane = tid & 63, g = lane >> 4, n = lane & 15;
  const int w = __builtin_amdgcn_readfirstlane(tid >> 6);

  const u32x2 xr = __builtin_amdgcn_raw_buffer_load_b64(mk(x, 4096), tid * 8, 0, 0);
  const int trow = xcd * 128 + w * 16 + g * 4;
  u32x4 WQ[4], K[4], V[4], W1[4], WO[4], W2[4];
  {
    const rsrc_t r = mk(wq, 1024 * 4096 * 2);
#pragma unroll
    for (int j = 0; j < 4; ++j) WQ[j] = ldw(r, (trow + j) * 8192 + jj * 256 + n * 16);
  }
  SB();
  const u32 kvbase = ((u32)(((xcd & 1) * 4 + (xcd >> 1)) * 32768 + 28672 + jj * 128 + w * 16)) * 256u;
  {
    const rsrc_t r = mk(kc, 2u * 4 * 32768 * 128 * 2);
#pragma unroll
    for (int t = 0; t < 4; ++t) K[t] = ldw(r, kvbase + n * 256 + t * 64 + g * 16);
  }
  SB();
  STAMP(1);
  const rsrc_t rw = mk(ws, WS_USED);
  Tag tag;
  u64 newep;
  {
    const u64 e = *(const __attribute__((address_space(4))) u64*)(uintptr_t)ws;
    const u64 t40 = ((e >> 24) * 6364136223846793005ull + 1442695040888963407ull) & 0xFFFFFFFFFFull;
    tag.hi = (u32)(t40 >> 8);
    tag.lo = (u32)(t40 & 0xFF);
    newep = t40 << 24;
  }
  u32 mk4[4];
#pragma unroll
  for (int i = 0; i < 4; ++i) mk4[i] = (i == (n >> 2)) ? ((n & 1) ? 0xFFFF0000u : 0x0000FFFFu) : 0u;
  const int mb = (n >> 1) & 1;

  lput<u32x2>(L_X + tid * 8, xr);
  lbar();
  u32x4 AX[4];
  {
    float v[32];
    const int hb = lane >> 5;
#pragma unroll
    for (int i = 0; i < 4; ++i) {
      const u32x4 t = lget<u32x4>(L_X + hb * 2048 + i * 512 + (lane & 31) * 16);
#pragma unroll
      for (int k = 0; k < 4; ++k) { v[i * 8 + 2 * k] = blo(t[k]); v[i * 8 + 2 * k + 1] = bhi(t[k]); }
    }
    const u32x2 xa = lget<u32x2>(L_X + mb * 2048 + trow * 2);
    float s0 = 0, s1 = 0, s2 = 0, s3 = 0;
#pragma unroll
    for (int i = 0; i < 32; i += 4) { s0 += v[i]; s1 += v[i + 1]; s2 += v[i + 2]; s3 += v[i + 3]; }
    const float mean = sum16(rowsum((s0 + s1) + (s2 + s3))) * (1.0f / 1024.0f);
    s0 = s1 = s2 = s3 = 0;
#pragma unroll
    for (int i = 0; i < 32; i += 4) {
      float d0 = v[i] - mean, d1 = v[i + 1] - mean, d2 = v[i + 2] - mean, d3 = v[i + 3] - mean;
      s0 += d0 * d0; s1 += d1 * d1; s2 += d2 * d2; s3 += d3 * d3;
    }
    const float var = sum16(rowsum((s0 + s1) + (s2 + s3))) * (1.0f / 1024.0f);
    const float mr = rbf(mean), den = rbf(__builtin_sqrtf(rbf(rbf(var) + 1e-5f)));
    const float m0 = asf(__builtin_amdgcn_readlane(asu(mr), 0)), m1 = asf(__builtin_amdgcn_readlane(asu(mr), 32));
    const float d0 = asf(__builtin_amdgcn_readlane(asu(den), 0)), d1 = asf(__builtin_amdgcn_readlane(asu(den), 32));
    const float mm = mb ? m1 : m0, dd = mb ? d1 : d0;
    AX[0] = onehot(dupbf(rbf(blo(xa[0]) - mm) / dd), mk4);
    AX[1] = onehot(dupbf(rbf(bhi(xa[0]) - mm) / dd), mk4);
    AX[2] = onehot(dupbf(rbf(blo(xa[1]) - mm) / dd), mk4);
    AX[3] = onehot(dupbf(rbf(bhi(xa[1]) - mm) / dd), mk4);
  }
  SB();
  STAMP(2);
  {
    const rsrc_t r = mk(vc, 2u * 4 * 32768 * 128 * 2);
    f32x4 acc = {0, 0, 0, 0};
#pragma unroll
    for (int j = 0; j < 4; ++j) {
      acc = mfma(AX[j], WQ[j], acc);
      SB();
      V[j] = ldw(r, kvbase + (g * 4 + j) * 256 + n * 16);
      SB();
    }
    STAMP(3);
    red_write(L_RQ, w, g, n, acc);
  }
  lbar();
  const int sub4 = lane & 3, pi = lane >> 2;
  {
    const int o = L_RQ + (w >> 2) * RPL + sub4 * 2 * RS + ((w & 3) * 16 + pi) * 8;
    const f32x2 a = lget<f32x2>(o), b = lget<f32x2>(o + RS);
    float u0 = a[0] + b[0], u1 = a[1] + b[1];
    u0 += dpp<XOR1>(u0); u1 += dpp<XOR1>(u1);
    u0 += dpp<XOR2>(u0); u1 += dpp<XOR2>(u1);
    stc(rw, sub4 == 0 ? QP_OFF + (jj * 8 + xcd) * 2048 + (w * 16 + pi) * 16 : OOB, pack2(u0, u1, tag));
  }
  SB();
  STAMP(4);
  pin(K[3]);
  SB();
  STAMP(5);
  const int hkv = xcd >> 1;
  const int cb1 = (jj + 1) & 31;
  const bool has1 = cb1 < 28;
  {
    u32x4 Q[8];
    const u32 qo = QP_OFF + ((hkv * 8 + w) * 8 * 2 + (xcd & 1)) * 1024 + lane * 16;
#pragma unroll
    for (int i = 0; i < 8; ++i) Q[i] = ldc(rw, qo + i * 2048);
    SB();
    {
      const rsrc_t r = mk(w1, has1 ? 1024 * 3584 * 2 : 0);
#pragma unroll
      for (int j = 0; j < 4; ++j) W1[j] = ldw(r, (trow + j) * 7168 + cb1 * 256 + n * 16);
    }
    SB();
    u32 bad = 0;
#pragma unroll
    for (int i = 0; i < 8; ++i) bad |= bad2(Q[i], tag);
    STAMP(6);
    for (u32 spin = 0; any_bad(bad) && spin < SPIN_CAP; ++spin) {
      asm volatile("" ::: "memory");
      bad = 0;
#pragma unroll
      for (int i = 0; i < 8; ++i) Q[i] = ldc(rw, qo + i * 2048);
#pragma unroll
      for (int i = 0; i < 8; ++i) bad |= bad2(Q[i], tag);
    }
    float q0 = 0, q1 = 0;
#pragma unroll
    for (int i = 0; i < 8; ++i) { q0 += pay(Q[i][0]); q1 += pay(Q[i][2]); }
    lput<u32>(L_Q + w * QS + lane * 4, (asu(rbf(q0)) >> 16) | asu(rbf(q1)));
  }
  lbar();
  STAMP(7);
  {
    f32x4 s = {0, 0, 0, 0};
#pragma unroll
    for (int t = 0; t < 4; ++t) s = mfma(K[t], lget<u32x4>(L_Q + (n & 7) * QS + t * 64 + g * 16), s);
    float sc[4];
#pragma unroll
    for (int r = 0; r < 4; ++r) sc[r] = rbf(rbf(s[r]) * 0.08838834764831845f);
    const float m = max32(max16(fmaxf(fmaxf(sc[0], sc[1]), fmaxf(sc[2], sc[3]))));
    u32x4 P;
    float l = 0;
    const u32 sh = (n & 8) ? 0u : 16u;
#pragma unroll
    for (int r = 0; r < 4; ++r) { const float e = __expf(sc[r] - m); l += e; P[r] = asu(rbf(e)) >> sh; }
    l = sum32(sum16(l));
    if (lane < 8) lput<f32x2>(L_ML + (w * 8 + lane) * 8, f32x2{m, l});
#pragma unroll
    for (int i = 0; i < 4; ++i) {
      const u32x4 B = {V[0][i], V[1][i], V[2][i], V[3][i]};
      const f32x4 o = mfma(P, B, f32x4{0, 0, 0, 0});
      lput<f32x4>(L_O + w * 4096 + (g & 1) * 2048 + (i * 32 + (g >> 1) * 16 + n) * 16, o);
    }
  }
  lbar();
  {
    const f32x2 ml = lget<f32x2>(L_ML + lane * 8);
    f32x2 ov[8];
    const int a = w >> 1;
#pragma unroll
    for (int i = 0; i < 8; ++i) ov[i] = lget<f32x2>(L_O + i * 4096 + (a >> 1) * 2048 + ((w & 1) * 64 + lane) * 16 + (a & 1) * 8);
    const float M = max32(max16(fmaxf(ml[0], dpp<ROR8>(ml[0]))));
    const float sc = __expf(ml[0] - M);
    float L = sc * ml[1];
    L += dpp<ROR8>(L);
    L = sum32(sum16(L));
    float o0 = 0, o1 = 0;
#pragma unroll
    for (int i = 0; i < 8; ++i) {
      o0 += asf(__builtin_amdgcn_readlane(asu(sc), i * 8 + 2 * a)) * ov[i][0];
      o1 += asf(__builtin_amdgcn_readlane(asu(sc), i * 8 + 2 * a + 1)) * ov[i][1];
    }
    stc(rw, PO_OFF + p * 8192 + w * 1024 + lane * 16, pack2(o0, o1, tag));
    stc(rw, (tid < 8) ? ST_OFF + p * 128 + lane * 16 : OOB, pack2(M, L, tag));
  }
  SB();
  STAMP(8);
  const int ks = p >> 2, ca = (ks >> 2) & 3, cc = ks & 3;
  {
    const rsrc_t r = mk(wo, 4096 * 1024 * 2);
    const int rg = w & 3;
    const int row0 = (hkv * 8 + 2 * ca + (rg >> 1)) * 128 + 32 * g + 2 * cc + (rg & 1);
#pragma unroll
    for (int j = 0; j < 4; ++j) WO[j] = ldw(r, (row0 + 8 * j) * 2048 + (p & 3) * 512 + (w >> 2) * 256 + n * 16);
  }
  SB();
  {
    f32x4 acc = {0, 0, 0, 0};
#pragma unroll
    for (int j = 0; j < 4; ++j) acc = mfma(AX[j], W1[j], acc);
    red_write(L_RH, w, g, n, acc);
  }
  STAMP(9);
  lbar();
  {
    const int o = L_RH + (w >> 2) * RPL + sub4 * 2 * RS + ((w & 3) * 16 + pi) * 8;
    const f32x2 a = lget<f32x2>(o), b = lget<f32x2>(o + RS);
    float u0 = a[0] + b[0], u1 = a[1] + b[1];
    u0 += dpp<XOR1>(u0); u1 += dpp<XOR1>(u1);
    u0 += dpp<XOR2>(u0); u1 += dpp<XOR2>(u1);
    stc(rw, (sub4 == 0 && has1) ? HP_OFF + (cb1 * 8 + xcd) * 2048 + (w * 16 + pi) * 16 : OOB, pack2(u0, u1, tag));
  }
  SB();
  const int j2 = (jj + 14) & 31;
  const bool has2 = j2 < 28;
  const int t2 = xcd * 28 + j2, k2 = t2 >> 3, cb2 = t2 & 7;
  u32x4 H[2];
  const u32 ho = has2 ? HP_OFF + ((k2 * 8 + 2 * (n >> 2) + (n & 1)) * 2 + mb) * 1024 + (w * 16 + g * 4) * 8 : OOB;
  {
    u32x4 PO[4], ST;
    const int pb = hkv * 64 + (w >> 2) * 32;
    const int grp = 2 * (w & 3) + (lane >> 5);
    const u32 po = PO_OFF + (pb + grp * 4) * 8192 + ca * 2048 + cc * 512 + (lane & 31) * 16;
    const u32 so = ST_OFF + (pb + (lane & 31)) * 128 + (2 * ca + (lane >> 5)) * 16;
    ST = ldc(rw, so);
#pragma unroll
    for (int i = 0; i < 4; ++i) PO[i] = ldc(rw, po + i * 8192);
    SB();
    {
      const rsrc_t r = mk(w2, has2 ? 3584 * 1024 * 2 : 0);
#pragma unroll
      for (int j = 0; j < 4; ++j) W2[j] = ldw(r, (k2 * 128 + w * 16 + g * 4 + j) * 2048 + cb2 * 256 + n * 16);
    }
    SB();
    u32 bad = bad2(ST, tag);
#pragma unroll
    for (int i = 0; i < 4; ++i) bad |= bad2(PO[i], tag);
    STAMP(10);
    for (u32 spin = 0; any_bad(bad) && spin < SPIN_CAP; ++spin) {
      asm volatile("" ::: "memory");
      ST = ldc(rw, so);
#pragma unroll
      for (int i = 0; i < 4; ++i) PO[i] = ldc(rw, po + i * 8192);
      bad = bad2(ST, tag);
#pragma unroll
      for (int i = 0; i < 4; ++i) bad |= bad2(PO[i], tag);
    }
    SB();
    H[0] = ldc(rw, ho);
    H[1] = ldc(rw, ho + 16);
    SB();
    const float m = pay(ST[0]);
    const float M = max16(rowmax(m));
    const float e = __expf(m - M);
    const float L = sum16(rowsum(e * pay(ST[2])));
    const u32 wn = asu(e / L);
    float o0 = 0, o1 = 0;
#pragma unroll
    for (int i = 0; i < 4; ++i) {
      o0 += asf(__builtin_amdgcn_ds_bpermute((grp * 4 + i) * 4, wn)) * pay(PO[i][0]);
      o1 += asf(__builtin_amdgcn_ds_bpermute((32 + grp * 4 + i) * 4, wn)) * pay(PO[i][2]);
    }
    auto sw = __builtin_amdgcn_permlane32_swap(asu(o0), asu(o1), false, false);
    lput<float>(L_PART + (w & 3) * 512 + (w >> 2) * 256 + lane * 4, asf(sw[0]) + asf(sw[1]));
  }
  lbar();
  STAMP(11);
  {
    f32x4 o = lget<f32x4>(L_PART + mb * 256 + ((w & 3) * 16 + g * 4) * 4);
#pragma unroll
    for (int i = 1; i < 4; ++i) o += lget<f32x4>(L_PART + i * 512 + mb * 256 + ((w & 3) * 16 + g * 4) * 4);
    f32x4 acc = {0, 0, 0, 0};
#pragma unroll
    for (int j = 0; j < 4; ++j) acc = mfma(onehot(dupbf(o[j]), mk4), WO[j], acc);
    red_write(L_RS, w, g, n, acc);
  }
  lbar();
  {
    const int P = tid >> 1, sub = lane & 1;
    const int ch = P >> 7, nn = (P >> 3) & 15, bb = (P >> 2) & 1, gg = P & 3;
    const int o = L_RS + bb * RPL + (ch * 4 + sub * 2) * RS + (nn * 4 + gg) * 8;
    const f32x2 a = lget<f32x2>(o), b = lget<f32x2>(o + RS);
    float u0 = a[0] + b[0], u1 = a[1] + b[1];
    u0 += dpp<XOR1>(u0); u1 += dpp<XOR1>(u1);
    stc(rw, sub == 0 ? SP_OFF + ks * 16384 + (p & 3) * 4096 + P * 16 : OOB, pack2(u0, u1, tag));
  }
  SB();
  STAMP(12);
  {
    u32 bad = has2 ? (bad2(H[0], tag) | bad2(H[1], tag)) : 0u;
    for (u32 spin = 0; any_bad(bad) && spin < SPIN_CAP; ++spin) {
      asm volatile("" ::: "memory");
      H[0] = ldc(rw, ho);
      H[1] = ldc(rw, ho + 16);
      bad = bad2(H[0], tag) | bad2(H[1], tag);
    }
    float h[4] = {pay(H[0][0]), pay(H[0][2]), pay(H[1][0]), pay(H[1][2])};
    f32x4 acc = {0, 0, 0, 0};
#pragma unroll
    for (int j = 0; j < 4; ++j) {
      float t = h[j];
      t += dpp<XOR1>(t); t += dpp<ROR4>(t); t += dpp<ROR8>(t);
      acc = mfma(onehot(dupbf(fmaxf(t, 0.0f)), mk4), W2[j], acc);
    }
    red_write(L_RG, w, g, n, acc);
  }
  STAMP(13);
  lbar();
  {
    const int P = w * 16 + pi;
    const int nn = P >> 3, bb = (P >> 2) & 1, gg = P & 3;
    const int o = L_RG + bb * RPL + sub4 * 2 * RS + (nn * 4 + gg) * 8;
    const f32x2 a = lget<f32x2>(o), b = lget<f32x2>(o + RS);
    float u0 = a[0] + b[0], u1 = a[1] + b[1];
    u0 += dpp<XOR1>(u0); u1 += dpp<XOR1>(u1);
    u0 += dpp<XOR2>(u0); u1 += dpp<XOR2>(u1);
    stc(rw, (sub4 == 0 && has2) ? GP_OFF + k2 * 16384 + cb2 * 2048 + P * 16 : OOB, pack2(u0, u1, tag));
  }
  SB();
  STAMP(14);
  if (w == 7 && (p & 1) == 0) {
    const int f = p >> 1, kg = lane >> 3, u = lane & 7;
    const u32 so = SP_OFF + kg * 16384 + f * 128 + u * 16;
    const u32 go = GP_OFF + kg * 16384 + f * 128 + u * 16;
    u32x4 S[8], G[4];
    u32 bad = 1;
    for (u32 spin = 0; any_bad(bad) && spin < SPIN_CAP; ++spin) {
      asm volatile("" ::: "memory");
#pragma unroll
      for (int i = 0; i < 8; ++i) S[i] = ldc(rw, so + i * 8 * 16384);
      bad = 0;
#pragma unroll
      for (int i = 0; i < 8; ++i) bad |= bad2(S[i], tag);
    }
    float s0 = 0, s1 = 0;
#pragma unroll
    for (int i = 0; i < 8; ++i) { s0 += pay(S[i][0]); s1 += pay(S[i][2]); }
    bad = 1;
    for (u32 spin = 0; any_bad(bad) && spin < SPIN_CAP; ++spin) {
      asm volatile("" ::: "memory");
#pragma unroll
      for (int i = 0; i < 4; ++i) G[i] = ldc(rw, (i < 3 || kg < 4) ? go + i * 8 * 16384 : OOB);
      bad = bad2(G[0], tag) | bad2(G[1], tag) | bad2(G[2], tag) | (kg < 4 ? bad2(G[3], tag) : 0u);
    }
    float g0 = 0, g1 = 0;
#pragma unroll
    for (int i = 0; i < 4; ++i) { g0 += pay(G[i][0]); g1 += pay(G[i][2]); }
    s0 += dpp<ROR8>(s0); s1 += dpp<ROR8>(s1); g0 += dpp<ROR8>(g0); g1 += dpp<ROR8>(g1);
    s0 = rbf(sum32(sum16(s0))); s1 = rbf(sum32(sum16(s1)));
    g0 = rbf(sum32(sum16(g0))); g1 = rbf(sum32(sum16(g1)));
    const int idx = (u >> 2) * 512 + f * 4 + (u & 3);
    const u32 xv = lget<u32>(L_X + idx * 4);
    const float r0 = rbf(s0 * rbf(g0 / (1.0f + __expf(-g0)))), r1 = rbf(s1 * rbf(g1 / (1.0f + __expf(-g1))));
    if (lane < 8) out[idx] = (asu(rbf(blo(xv) + r0)) >> 16) | asu(rbf(bhi(xv) + r1));
    if (p == 0 && lane == 0) __hip_atomic_store((u64*)ws, newep, __ATOMIC_RELAXED, __HIP_MEMORY_SCOPE_AGENT);
  }
}

__global__ __launch_bounds__(512, 2) void hold_kernel(int ticks) {
  const u64 tt0 = __builtin_amdgcn_s_memrealtime();
  for (int i = 0; i < 100000 && __builtin_amdgcn_s_memrealtime() - tt0 < (u64)ticks; ++i) __builtin_amdgcn_s_sleep(1);
}
extern "C" void kernel_launch(void* const* d_in, const int* in_sizes, int n_in, void* d_out, int out_size, void* d_ws,
                              size_t ws_size, hipStream_t stream) {
  (void)in_sizes; (void)n_in; (void)out_size; (void)ws_size;
  (void)hipFuncSetAttribute((const void*)fused_kernel, hipFuncAttributeMaxDynamicSharedMemorySize, LDS_BYTES);
  fused_kernel<<<256, 512, LDS_BYTES, stream>>>(d_in[0], d_in[1], d_in[2], d_in[3], d_in[4], d_in[6], d_in[7], (u32*)d_out, d_ws);
  hold_kernel<<<1, 64, 0, stream>>>(1000);
}
```

```cpp
#include <hip/hip_runtime.h>
#include <hip/hip_bf16.h>
#include <stdint.h>

typedef unsigned int u32;
typedef unsigned long long u64;
typedef u32 u32x4 __attribute__((ext_vector_type(4)));
typedef u32 u32x2 __attribute__((ext_vector_type(2)));
typedef float f32x4 __attribute__((ext_vector_type(4)));
typedef float f32x2 __attribute__((ext_vector_type(2)));
typedef short bf16x8 __attribute__((ext_vector_type(8)));
typedef __bf16 bf16x2 __attribute__((ext_vector_type(2)));
typedef __amdgpu_buffer_rsrc_t rsrc_t;
#define DI __device__ __forceinline__
#define SB() __builtin_amdgcn_sched_barrier(0)
#define UNLIKELY(x) __builtin_expect(!!(x), 0)

constexpr u32 QP_OFF = 4096;
constexpr u32 HP_OFF = QP_OFF + 32 * 2 * 8 * 512;
constexpr u32 PO_OFF = HP_OFF + 28 * 8 * 2 * 512;
constexpr u32 ML_OFF = PO_OFF + 256 * 4096;
constexpr u32 SP_OFF = ML_OFF + 256 * 128;
constexpr u32 GP_OFF = SP_OFF + 64 * 8192;
constexpr u32 WS_USED = GP_OFF + 28 * 8192;
constexpr u32 OOB = 0x80000000u;
constexpr u32 SPIN_CAP = 1u << 16;
#define STAMP(i) do { } while (0)

constexpr int L_ST = 0;
constexpr int L_RQ = 256;
constexpr int L_RH = L_RQ + 4096;
constexpr int L_RS = L_RH + 4096;
constexpr int L_RG = L_RS + 4 * 3072;
constexpr int L_Q = L_RG + 4 * 1280;
constexpr int QS = 272;
constexpr int L_ML = L_Q + 8 * QS;
constexpr int L_O = L_ML + 256;
constexpr int L_END = L_O + 4 * 4096;
constexpr int LDS_BYTES = 84 * 1024;
static_assert(L_END <= LDS_BYTES, "lds");

DI rsrc_t mk(const void* p, u32 off, u32 bytes) { return __builtin_amdgcn_make_buffer_rsrc((char*)p + off, 0, bytes, 0x00020000); }
DI u32x4 ldw(rsrc_t r, u32 v, u32 s) { return __builtin_amdgcn_raw_buffer_load_b128(r, v, s, 2); }
DI u32x4 ldc(rsrc_t r, u32 v, u32 s) { return __builtin_amdgcn_raw_buffer_load_b128(r, v, s, 16); }
DI void stc4(rsrc_t r, u32 off, u32x4 v) { __builtin_amdgcn_raw_buffer_store_b128(v, r, off, 0, 16); }
DI void stc2(rsrc_t r, u32 off, u32x2 v) { __builtin_amdgcn_raw_buffer_store_b64(v, r, off, 0, 16); }
DI void stc1(rsrc_t r, u32 off, u32 v) { __builtin_amdgcn_raw_buffer_store_b32(v, r, off, 0, 16); }
DI float asf(u32 u) { return __builtin_bit_cast(float, u); }
DI u32 asu(float f) { return __builtin_bit_cast(u32, f); }
DI u32 cvt2(float a, float b) { return __builtin_bit_cast(u32, __builtin_convertvector(f32x2{a, b}, bf16x2)); }
DI float blo(u32 u) { return asf(u << 16); }
DI float bhi(u32 u) { return asf(u & 0xFFFF0000u); }
DI float rbf(float f) { return blo(cvt2(f, f)); }
DI f32x4 mfma(u32x4 a, u32x4 b, f32x4 c) {
  return __builtin_amdgcn_mfma_f32_16x16x32_bf16(__builtin_bit_cast(bf16x8, a), __builtin_bit_cast(bf16x8, b), c, 0, 0, 0);
}
template <int CTRL> DI float dpp(float x) {
  return __builtin_bit_cast(float, __builtin_amdgcn_mov_dpp(__builtin_bit_cast(int, x), CTRL, 0xf, 0xf, true));
}
constexpr int XOR1 = 0xB1, XOR2 = 0x4E, ROR8 = 0x128, ROR4 = 0x124, HMIR = 0x141;
DI float tr32(float a, float b) { auto s = __builtin_amdgcn_permlane32_swap(asu(a), asu(b), false, false); return asf(s[0]) + asf(s[1]); }
DI float tr16(float a, float b) { auto s = __builtin_amdgcn_permlane16_swap(asu(a), asu(b), false, false); return asf(s[0]) + asf(s[1]); }
DI float max16(float x) { auto s = __builtin_amdgcn_permlane16_swap(asu(x), asu(x), false, false); return fmaxf(asf(s[0]), asf(s[1])); }
DI float max32(float x) { auto s = __builtin_amdgcn_permlane32_swap(asu(x), asu(x), false, false); return fmaxf(asf(s[0]), asf(s[1])); }
DI float sum8(float x) { x += dpp<XOR1>(x); x += dpp<XOR2>(x); x += dpp<HMIR>(x); return x; }
DI float max8(float x) { x = fmaxf(x, dpp<XOR1>(x)); x = fmaxf(x, dpp<XOR2>(x)); x = fmaxf(x, dpp<HMIR>(x)); return x; }
DI void lbar() { asm volatile("s_waitcnt lgkmcnt(0)" ::: "memory"); __builtin_amdgcn_s_barrier(); asm volatile("" ::: "memory"); }
DI void pin(u32x4 v) { asm volatile("" ::"v"(v)); }
DI void keep(u32& v) { asm volatile("" : "+v"(v)); }
DI void keep4(u32x4& v) { asm volatile("" : "+v"(v)); }
DI float ex2(float x) { return __builtin_amdgcn_exp2f(x); }
constexpr float LOG2E = 1.4426950408889634f;

DI u32 pk(float a, u32 tag) { return ((asu(a) + 0x800u) & 0xFFFFF000u) | tag; }
DI bool any_bad(u32 acc) { return __builtin_amdgcn_ballot_w64((acc & 0xFFFu) != 0) != 0; }
DI u32 or4(u32x4 v) { return (v[0] | v[1]) | (v[2] | v[3]); }
DI u32x4 onehot(u32 dup, const u32x4 mk4) { return u32x4{dup, dup, dup, dup} & mk4; }

extern __shared__ __attribute__((aligned(16))) char smem[];
template <class T> DI T lget(int off) { return *(const T*)(smem + off); }
template <class T> DI void lput(int off, T v) { *(T*)(smem + off) = v; }

__global__ __launch_bounds__(256, 2) void fused_kernel(const void* __restrict__ x, const void* __restrict__ kc, const void* __restrict__ vc,
                                                       const void* __restrict__ wq, const void* __restrict__ wo, const void* __restrict__ w1,
                                                       const void* __restrict__ w2, u32* __restrict__ out, void* ws) {
  STAMP(0);
  const u32 bid = blockIdx.x;
  const u32 xcd = bid & 7, jj = bid >> 3, p = xcd * 32 + jj;
  const u32 tid = threadIdx.x, lane = tid & 63, g = lane >> 4, n = lane & 15;
  const u32 w = __builtin_amdgcn_readfirstlane(tid >> 6);

  u32x4 WQ[8], K[8], V[8], W1[8], WO[8], W2[8];
  const rsrc_t rx = mk(x, 0, 4096);
  const u32x4 xs = __builtin_amdgcn_raw_buffer_load_b128(rx, tid * 16, 0, 0);
  const u32x4 xa = __builtin_amdgcn_raw_buffer_load_b128(rx, (n >> 3) * 2048 + g * 16, xcd * 256 + w * 64, 0);
  {
    const rsrc_t r = mk(wq, (xcd * 128 + w * 32) * 8192 + jj * 256, 32 * 8192 - 7936);
    const u32 vo = g * 65536 + n * 16;
#pragma unroll
    for (int j = 0; j < 8; ++j) WQ[j] = ldw(r, vo, j * 8192);
  }
  SB();
  STAMP(1);
  const u32 xf = __builtin_amdgcn_raw_buffer_load_b32(rx, (n & 1) * 2048 + (g & 1) * 4, p * 8, 0);
  const rsrc_t rw = mk(ws, 0, WS_USED);
  const u32 tag = (*(const __attribute__((address_space(4))) u32*)(uintptr_t)ws + 1u) & 0xFFFu;
  const u32x4 tag4 = {tag, tag, tag, tag};
  const u32 hkv = xcd >> 1;
  const u32 kvb = (((xcd & 1) * 4 + hkv) * 32768 + 28672 + jj * 128 + w * 32) * 256u;
  const rsrc_t rk = mk(kc, kvb, 8192), rv = mk(vc, kvb, 8192);
  const u32 cb1 = (jj + 1) & 31;
  const bool has1 = cb1 < 28;
  const rsrc_t r1 = mk(w1, has1 ? (xcd * 128 + w * 32) * 7168 + cb1 * 256 : 0, has1 ? 32 * 7168 - 6912 : 0);
  const u32 ks = p >> 2, chead = (ks >> 1) & 7, ce = ks & 1, cg = p & 3;
  const rsrc_t ro = mk(wo, ((hkv * 8 + chead) * 128 + 2 * w + ce) * 2048 + cg * 512, 120 * 2048 + 512);
  const u32 j2 = (jj + 14) & 31;
  const bool has2 = j2 < 28;
  const u32 t2 = xcd * 28 + j2, k2 = t2 >> 3, cb2 = t2 & 7;
  const rsrc_t r2 = mk(w2, has2 ? (k2 * 128 + w * 32) * 2048 + cb2 * 256 : 0, has2 ? 32 * 2048 - 1792 : 0);
  u32 vk = (4 * (n & 3) + (n >> 2)) * 256 + g * 16, vv = lane * 16, v1 = g * 57344 + n * 16, vo_ = g * 65536 + n * 16, v2 = g * 16384 + n * 16;
  auto LK = [&](int j) { SB(); K[j] = ldw(rk, vk + (j >> 1) * 64, (j & 1) * 4096); SB(); };
  auto LV = [&](int j) { SB(); V[j] = ldw(rv, vv + (j >> 1) * 1024, (j & 1) * 4096); SB(); };
  auto L1 = [&](int j) { SB(); W1[j] = ldw(r1, v1, j * 7168); SB(); };
  auto LO = [&](int j) { SB(); WO[j] = ldw(ro, vo_ + (j & 1) * 256, (j >> 1) * 16384); SB(); };
  auto L2 = [&](int j) { SB(); W2[j] = ldw(r2, v2, j * 2048); SB(); };
  u32 a_qst = QP_OFF + ((jj * 2 + (w >> 1)) * 8 + xcd) * 512 + (w & 1) * 256 + n * 16 + g * 4;
  u32 a_qld = QP_OFF + ((hkv * 8 + 2 * w + (lane >> 5)) * 2 + (xcd & 1)) * 4096 + (lane & 31) * 16;
  u32 a_post = PO_OFF + p * 4096 + w * 1024 + (n & 7) * 128 + (n >> 3) * 64 + g * 16;
  u32 a_mlst = tid < 8 ? ML_OFF + p * 128 + lane * 16 : OOB;
  u32 a_hst = has1 ? HP_OFF + ((cb1 * 8 + xcd) * 2 + (w >> 1)) * 512 + (w & 1) * 256 + n * 16 + g * 4 : OOB;
  u32 a_pold = PO_OFF + (hkv * 64 + 4 * n) * 4096 + w * 1024 + chead * 128 + ce * 64 + g * 16;
  u32 a_mlld = ML_OFF + (hkv * 64 + 4 * n + g) * 128 + chead * 16;
  u32 a_sst = SP_OFF + ks * 8192 + cg * 2048 + w * 512 + n * 32 + g * 8;
  u32 a_hld = has2 ? HP_OFF + ((k2 * 8 + (n & 7)) * 2 + (n >> 3)) * 512 + (4 * w + g) * 16 : OOB;
  u32 a_gst = has2 ? GP_OFF + k2 * 8192 + cb2 * 1024 + w * 256 + n * 16 + g * 4 : OOB;
  u32 a_fs = SP_OFF + (lane >> 1) * 8192 + p * 32 + (lane & 1) * 16;
  u32 a_fg = lane < 56 ? a_fs + 64 * 8192 : OOB;
  u32x4 mk4;
#pragma unroll
  for (u32 i = 0; i < 4; ++i) mk4[i] = (i == ((n & 7) >> 1)) ? ((n & 1) ? 0xFFFF0000u : 0x0000FFFFu) : 0u;
  keep(vk); keep(vv); keep(v1); keep(vo_); keep(v2);
  keep(a_qst); keep(a_qld); keep(a_post); keep(a_mlst); keep(a_hst); keep(a_pold); keep(a_mlld);
  keep(a_sst); keep(a_hld); keep(a_gst); keep(a_fs); keep(a_fg); keep4(mk4);
  SB();

  u32x4 AX[8];
  {
    const float pv = asf(__builtin_amdgcn_readfirstlane(xs[0]) << 16);
    float d[8];
#pragma unroll
    for (int k = 0; k < 4; ++k) { d[2 * k] = blo(xs[k]) - pv; d[2 * k + 1] = bhi(xs[k]) - pv; }
    const float s1 = ((d[0] + d[1]) + (d[2] + d[3])) + ((d[4] + d[5]) + (d[6] + d[7]));
    const float s2 = (d[0] * d[0] + d[1] * d[1] + d[2] * d[2] + d[3] * d[3]) + (d[4] * d[4] + d[5] * d[5] + d[6] * d[6] + d[7] * d[7]);
    float t = tr32(s1, s2);
    t = tr16(t, t);
    t += dpp<XOR1>(t); t += dpp<XOR2>(t); t += dpp<ROR4>(t); t += dpp<ROR8>(t);
    if ((lane & 31) == 0) lput<f32x2>(L_ST + w * 16 + (lane >> 5) * 8, f32x2{t, pv});
    lbar();
    const f32x4 a = lget<f32x4>(L_ST + (n >> 3) * 32), b = lget<f32x4>(L_ST + (n >> 3) * 32 + 16);
    const float da = a[0] * (1.0f / 512.0f), db = b[0] * (1.0f / 512.0f);
    const float ma = a[1] + da, mb = b[1] + db, dm = ma - mb;
    const float m2 = (a[2] - a[0] * da) + (b[2] - b[0] * db) + dm * dm * 256.0f;
    const u32 mv = cvt2((ma + mb) * 0.5f, fmaxf(m2, 0.0f) * (1.0f / 1024.0f));
    const float mr = blo(mv);
    const float rd = __builtin_amdgcn_rcpf(rbf(__builtin_amdgcn_sqrtf(rbf(bhi(mv) + 1e-5f))));
#pragma unroll
    for (int k = 0; k < 4; ++k) {
      const u32 c = cvt2(blo(xa[k]) - mr, bhi(xa[k]) - mr);
      const float e0 = blo(c) * rd, e1 = bhi(c) * rd;
      AX[2 * k] = onehot(cvt2(e0, e0), mk4);
      AX[2 * k + 1] = onehot(cvt2(e1, e1), mk4);
    }
#pragma unroll
    for (int j = 0; j < 8; ++j) keep4(AX[j]);
  }
  STAMP(2);
  {
    f32x4 acc = {0, 0, 0, 0};
#pragma unroll
    for (int j = 0; j < 8; ++j) {
      pin(WQ[j]);
      if (j >= 2) LK(j - 2);
      acc = mfma(AX[j], WQ[j], acc);
      SB();
    }
    STAMP(3);
    lput<f32x4>(L_RQ + tid * 16, acc);
    lbar();
    const f32x4 v = lget<f32x4>(L_RQ + g * 1024 + w * 256 + n * 16);
    stc1(rw, a_qst, pk(tr16(tr32(v[0], v[2]), tr32(v[1], v[3])), tag));
  }
  SB();
  STAMP(4);
  pin(K[0]); LK(6);
  pin(K[1]); LK(7);
#pragma unroll
  for (int j = 2; j < 8; ++j) { pin(K[j]); LV(j - 2); }
  STAMP(5);
  u32x4 VT[2][4];
  {
    u32x4 Q[8];
#pragma unroll
    for (int i = 0; i < 8; ++i) Q[i] = ldc(rw, a_qld + i * 512, 0);
    SB();
    pin(V[0]); LV(6);
    pin(V[1]); LV(7);
#pragma unroll
    for (int j = 2; j < 6; ++j) { pin(V[j]); L1(j - 2); }
#pragma unroll
    for (int a = 0; a < 2; ++a)
#pragma unroll
      for (int i = 0; i < 4; ++i) { VT[a][i] = u32x4{V[a][i], V[2 + a][i], V[4 + a][i], 0}; keep4(VT[a][i]); }
    SB();
    f32x4 q;
    auto qred = [&]() __attribute__((always_inline)) -> u32 {
      u32x4 e = Q[0] ^ tag4;
      u32 bad = or4(e);
      q = __builtin_bit_cast(f32x4, e);
#pragma unroll
      for (int i = 1; i < 8; ++i) { e = Q[i] ^ tag4; bad |= or4(e); q += __builtin_bit_cast(f32x4, e); }
      asm volatile("" : "+v"(q));
      return bad;
    };
    const u32 bad0 = qred();
    STAMP(6);
    if (UNLIKELY(any_bad(bad0))) {
      u32 spin = 0, bad;
      do {
        asm volatile("" ::: "memory");
#pragma unroll
        for (int i = 0; i < 8; ++i) Q[i] = ldc(rw, a_qld + i * 512, 0);
        bad = qred();
      } while (any_bad(bad) && ++spin < SPIN_CAP);
    }
    lput<u32x2>(L_Q + (2 * w + (lane >> 5)) * QS + n * 16 + (g & 1) * 8, u32x2{cvt2(q[0], q[1]), cvt2(q[2], q[3])});
  }
  L1(4);
  lbar();
  STAMP(7);
  {
    f32x4 s0 = {0, 0, 0, 0}, s1 = {0, 0, 0, 0};
#pragma unroll
    for (int t = 0; t < 4; ++t) {
      const u32x4 qf = lget<u32x4>(L_Q + (n & 7) * QS + t * 64 + g * 16);
      s0 = mfma(K[2 * t], qf, s0);
      s1 = mfma(K[2 * t + 1], qf, s1);
    }
    L1(5);
    float sc[8];
#pragma unroll
    for (int r = 0; r < 4; ++r) {
      const u32 c = cvt2(s0[r], s1[r]);
      const u32 e = cvt2(blo(c) * 0.08838834764831845f, bhi(c) * 0.08838834764831845f);
      sc[r] = blo(e); sc[4 + r] = bhi(e);
    }
    const float m = max32(max16(fmaxf(fmaxf(fmaxf(sc[0], sc[1]), fmaxf(sc[2], sc[3])), fmaxf(fmaxf(sc[4], sc[5]), fmaxf(sc[6], sc[7])))));
    const float mc = m * LOG2E;
    const u32 pm = (n & 8) ? 0xFFFF0000u : 0x0000FFFFu;
    u32x4 P0, P1;
    float l = 0;
#pragma unroll
    for (int r = 0; r < 4; ++r) {
      const float e0 = ex2(__builtin_fmaf(sc[r], LOG2E, -mc)), e1 = ex2(__builtin_fmaf(sc[4 + r], LOG2E, -mc));
      l += e0 + e1;
      P0[r] = cvt2(e0, e0) & pm;
      P1[r] = cvt2(e1, e1) & pm;
    }
    SB();
    pin(V[7]);
    L1(6);
#pragma unroll
    for (int i = 0; i < 4; ++i) {
      VT[0][i][3] = V[6][i];
      VT[1][i][3] = V[7][i];
      const f32x4 o = mfma(VT[1][i], P1, mfma(VT[0][i], P0, f32x4{0, 0, 0, 0}));
      lput<f32x4>(L_O + w * 4096 + i * 1024 + lane * 16, o);
    }
    l = tr16(l, l);
    l = tr32(l, l);
    if ((lane & 31) < 8) lput<float>(L_ML + (lane & 7) * 32 + (lane >> 5) * 16 + w * 4, lane < 32 ? m : l);
  }
  L1(7);
  lbar();
  {
    const f32x4 m4 = lget<f32x4>(L_ML + (n & 7) * 32), l4 = lget<f32x4>(L_ML + (n & 7) * 32 + 16);
    f32x4 ov[4];
#pragma unroll
    for (int i = 0; i < 4; ++i) ov[i] = lget<f32x4>(L_O + i * 4096 + w * 1024 + lane * 16);
    const float M = fmaxf(fmaxf(m4[0], m4[1]), fmaxf(m4[2], m4[3]));
    float f[4];
#pragma unroll
    for (int i = 0; i < 4; ++i) f[i] = ex2((m4[i] - M) * LOG2E);
    u32x4 st;
#pragma unroll
    for (int r = 0; r < 4; ++r) st[r] = pk(f[0] * ov[0][r] + f[1] * ov[1][r] + f[2] * ov[2][r] + f[3] * ov[3][r], tag);
    stc4(rw, a_post, st);
    const float L = f[0] * l4[0] + f[1] * l4[1] + f[2] * l4[2] + f[3] * l4[3];
    stc4(rw, a_mlst, u32x4{asu(M) | tag, pk(L, tag), tag, tag});
  }
  SB();
  STAMP(8);
  LO(0);
  {
    f32x4 acc = {0, 0, 0, 0};
#pragma unroll
    for (int j = 0; j < 8; ++j) {
      pin(W1[j]);
      if (j >= 3) LO(j - 2);
      acc = mfma(AX[j], W1[j], acc);
      SB();
    }
    STAMP(9);
    lput<f32x4>(L_RH + tid * 16, acc);
    lbar();
    const f32x4 v = lget<f32x4>(L_RH + g * 1024 + w * 256 + n * 16);
    stc1(rw, a_hst, pk(tr16(tr32(v[0], v[2]), tr32(v[1], v[3])), tag));
  }
  SB();
  STAMP(10);
  u32x4 H[2], H2[2];
  {
    u32x4 PA[4], PB[4];
    u32x4 MA, MB;
    MA = ldc(rw, a_mlld, 0);
#pragma unroll
    for (int i = 0; i < 4; ++i) PA[i] = ldc(rw, a_pold, i * 4096);
    SB();
    pin(WO[0]); LO(6);
    pin(WO[1]); LO(7);
    pin(WO[2]); L2(0);
    MB = ldc(rw, a_mlld, 0);
#pragma unroll
    for (int i = 0; i < 4; ++i) PB[i] = ldc(rw, a_pold, i * 4096);
    SB();
#pragma unroll
    for (int j = 1; j < 8; ++j) L2(j);
    u32 od[4];
    auto ored = [&](const u32x4 (&P)[4], u32x4 ML) __attribute__((always_inline)) -> u32 {
      ML ^= tag4;
      u32 bad = or4(ML);
      const float m = asf(ML[0]);
      const float M = max32(max16(max8(m)));
      const float e = ex2((m - M) * LOG2E);
      float L = sum8(e * asf(ML[1]));
      L = tr16(L, L);
      L = tr32(L, L);
      const float rl = __builtin_amdgcn_rcpf(L);
      auto s16 = __builtin_amdgcn_permlane16_swap(asu(e), asu(e), false, false);
      auto sa = __builtin_amdgcn_permlane32_swap(s16[0], s16[0], false, false);
      auto sb = __builtin_amdgcn_permlane32_swap(s16[1], s16[1], false, false);
      const float ei[4] = {asf(sa[0]), asf(sb[0]), asf(sa[1]), asf(sb[1])};
      float o[4] = {0, 0, 0, 0};
#pragma unroll
      for (int i = 0; i < 4; ++i) {
        const u32x4 t = P[i] ^ tag4;
        bad |= or4(t);
#pragma unroll
        for (int r = 0; r < 4; ++r) o[r] = i ? __builtin_fmaf(ei[i], asf(t[r]), o[r]) : ei[0] * asf(t[r]);
      }
#pragma unroll
      for (int r = 0; r < 4; ++r) { const float t = sum8(o[r]) * rl; od[r] = cvt2(t, t); keep(od[r]); }
      return bad;
    };
    if (UNLIKELY(any_bad(ored(PA, MA)))) {
      asm volatile("" ::: "memory");
      if (any_bad(ored(PB, MB))) {
        u32 spin = 0, bad;
        do {
          asm volatile("" ::: "memory");
          MA = ldc(rw, a_mlld, 0);
#pragma unroll
          for (int i = 0; i < 4; ++i) PA[i] = ldc(rw, a_pold, i * 4096);
          bad = ored(PA, MA);
        } while (any_bad(bad) && ++spin < SPIN_CAP);
      }
    }
    SB();
    STAMP(11);
    H[0] = ldc(rw, a_hld, 0);
    H[1] = ldc(rw, a_hld + 256, 0);
    SB();
    f32x4 a0 = {0, 0, 0, 0}, a1 = {0, 0, 0, 0};
    u32x4 AO[4];
#pragma unroll
    for (int r = 0; r < 4; ++r) AO[r] = onehot(od[r], mk4);
#pragma unroll
    for (int r = 0; r < 4; ++r) {
      a0 = mfma(AO[r], WO[2 * r], a0);
      a1 = mfma(AO[r], WO[2 * r + 1], a1);
    }
    lput<f32x4>(L_RS + w * 3072 + g * 384 + n * 16, a0);
    lput<f32x4>(L_RS + w * 3072 + 1536 + g * 384 + n * 16, a1);
    lbar();
    const int ra = L_RS + g * 3072 + (w >> 1) * 1536 + (n & 1) * 384 + (8 * (w & 1) + (n >> 1)) * 16;
    const f32x4 va = lget<f32x4>(ra), vb = lget<f32x4>(ra + 768);
    const float u0 = tr32(va[0], vb[0]), u1 = tr32(va[1], vb[1]), u2 = tr32(va[2], vb[2]), u3 = tr32(va[3], vb[3]);
    stc2(rw, a_sst, u32x2{pk(tr16(u0, u2), tag), pk(tr16(u1, u3), tag)});
    SB();
    H2[0] = ldc(rw, a_hld, 0);
    H2[1] = ldc(rw, a_hld + 256, 0);
    SB();
    pin(MB); pin(PB[0]); pin(PB[1]); pin(PB[2]); pin(PB[3]);
  }
  SB();
  STAMP(12);
  if (has2) {
    u32 hd[8];
    auto hred = [&](const u32x4 (&X)[2]) __attribute__((always_inline)) -> u32 {
      const u32x4 t0 = X[0] ^ tag4, t1 = X[1] ^ tag4;
#pragma unroll
      for (int j = 0; j < 8; ++j) { const float t = fmaxf(sum8(asf(j < 4 ? t0[j & 3] : t1[j & 3])), 0.0f); hd[j] = cvt2(t, t); keep(hd[j]); }
      return or4(t0) | or4(t1);
    };
    if (UNLIKELY(any_bad(hred(H)))) {
      asm volatile("" ::: "memory");
      if (any_bad(hred(H2))) {
        u32 spin = 0, bad;
        do {
          asm volatile("" ::: "memory");
          H[0] = ldc(rw, a_hld, 0);
          H[1] = ldc(rw, a_hld + 256, 0);
          bad = hred(H);
        } while (any_bad(bad) && ++spin < SPIN_CAP);
      }
    }
    STAMP(13);
    f32x4 acc = {0, 0, 0, 0};
#pragma unroll
    for (int j = 0; j < 8; ++j) acc = mfma(onehot(hd[j], mk4), W2[j], acc);
    lput<f32x4>(L_RG + w * 1280 + g * 320 + n * 16, acc);
    lbar();
    const f32x4 v = lget<f32x4>(L_RG + g * 1280 + (2 * (n & 1) + ((n >> 1) & 1)) * 320 + (4 * w + (n >> 2)) * 16);
    stc1(rw, a_gst, pk(tr16(tr32(v[0], v[2]), tr32(v[1], v[3])), tag));
  }
  SB();
  STAMP(14);
  if (w != 0) { pin(H2[0]); pin(H2[1]); }
  if (w == 0) {
    u32x4 S0, S1, G;
    const u32 gt = lane < 56 ? tag : 0u;
    u32 bad, spin = 0;
    do {
      asm volatile("" ::: "memory");
      S0 = ldc(rw, a_fs, 0);
      S1 = ldc(rw, a_fs, 32 * 8192);
      G = ldc(rw, a_fg, 0);
      S0 ^= tag4; S1 ^= tag4; G ^= u32x4{gt, gt, gt, gt};
      bad = or4(S0) | or4(S1) | or4(G);
    } while (any_bad(bad) && ++spin < SPIN_CAP);
    pin(H2[0]); pin(H2[1]);
    float u[4];
#pragma unroll
    for (int r = 0; r < 4; ++r) u[r] = tr32(asf(S0[r]) + asf(S1[r]), asf(G[r]));
    float y0 = tr16(u[0], u[2]), y1 = tr16(u[1], u[3]);
    y0 += dpp<XOR2>(y0); y1 += dpp<XOR2>(y1);
    y0 += dpp<ROR4>(y0); y1 += dpp<ROR4>(y1);
    y0 += dpp<ROR8>(y0); y1 += dpp<ROR8>(y1);
    const u32 yb = cvt2(y0, y1);
    auto sw = __builtin_amdgcn_permlane32_swap(yb, yb, false, false);
    const float g0 = blo(sw[1]), g1 = bhi(sw[1]);
    const u32 sg = cvt2(g0 * __builtin_amdgcn_rcpf(1.0f + ex2(-LOG2E * g0)), g1 * __builtin_amdgcn_rcpf(1.0f + ex2(-LOG2E * g1)));
    const u32 pr = cvt2(blo(sw[0]) * blo(sg), bhi(sw[0]) * bhi(sg));
    const u32 res = cvt2(blo(xf) + blo(pr), bhi(xf) + bhi(pr));
    if ((lane & 46) == 0) out[(n & 1) * 512 + p * 2 + (g & 1)] = res;
    if (p == 0 && lane == 0) __hip_atomic_store((u32*)ws, tag, __ATOMIC_RELAXED, __HIP_MEMORY_SCOPE_AGENT);
  }
}

extern "C" void kernel_launch(void* const* d_in, const int* in_sizes, int n_in, void* d_out, int out_size, void* d_ws,
                              size_t ws_size, hipStream_t stream) {
  (void)in_sizes; (void)n_in; (void)out_size; (void)ws_size;
  (void)hipFuncSetAttribute((const void*)fused_kernel, hipFuncAttributeMaxDynamicSharedMemorySize, LDS_BYTES);
  fused_kernel<<<256, 256, LDS_BYTES, stream>>>(d_in[0], d_in[1], d_in[2], d_in[3], d_in[4], d_in[6], d_in[7], (u32*)d_out, d_ws);
}
```

```cpp
#include <hip/hip_runtime.h>
#include <hip/hip_bf16.h>
#include <stdint.h>

typedef unsigned int u32;
typedef unsigned long long u64;
typedef u32 u32x4 __attribute__((ext_vector_type(4)));
typedef u32 u32x2 __attribute__((ext_vector_type(2)));
typedef float f32x4 __attribute__((ext_vector_type(4)));
typedef float f32x2 __attribute__((ext_vector_type(2)));
typedef short bf16x8 __attribute__((ext_vector_type(8)));
typedef __bf16 bf16x2 __attribute__((ext_vector_type(2)));
typedef __amdgpu_buffer_rsrc_t rsrc_t;
#define DI __device__ __forceinline__
#define SB() __builtin_amdgcn_sched_barrier(0)
#define UNLIKELY(x) __builtin_expect(!!(x), 0)

constexpr u32 QP_OFF = 4096;
constexpr u32 HP_OFF = QP_OFF + 32 * 2 * 8 * 512;
constexpr u32 PO_OFF = HP_OFF + 112 * 2048;
constexpr u32 ML_OFF = PO_OFF + 256 * 4096;
constexpr u32 SP_OFF = ML_OFF + 32 * 1024;
constexpr u32 GP_OFF = SP_OFF + 128 * 8192;
constexpr u32 WS_USED = GP_OFF + 112 * 8192;
#define POPOS 2
#define POBURST 8
constexpr u32 OOB = 0x80000000u;
constexpr u32 SPIN_CAP = 1u << 16;
#define STAMP(i) do { } while (0)

constexpr int L_ST = 0;
constexpr int L_PX = 256;
constexpr int L_RQ = 1024;
constexpr int L_RH = L_RQ + 4096;
constexpr int L_Q = L_RH + 4096;
constexpr int L_ML = L_Q + 2304;
constexpr int L_O = L_ML + 256;
constexpr int L_END = L_O + 4 * 4096;
constexpr int LDS_BYTES = 84 * 1024;
static_assert(L_END <= LDS_BYTES, "lds");

DI rsrc_t mk(const void* p, u32 off, u32 bytes) { return __builtin_amdgcn_make_buffer_rsrc((char*)p + off, 0, bytes, 0x00020000); }
DI u32x4 ldw(rsrc_t r, u32 v, u32 s) { return __builtin_amdgcn_raw_buffer_load_b128(r, v, s, 2); }
DI u32x4 ldc(rsrc_t r, u32 v, u32 s) { return __builtin_amdgcn_raw_buffer_load_b128(r, v, s, 16); }
DI void stc4(rsrc_t r, u32 off, u32x4 v) { __builtin_amdgcn_raw_buffer_store_b128(v, r, off, 0, 16); }
DI void stc2(rsrc_t r, u32 off, u32x2 v) { __builtin_amdgcn_raw_buffer_store_b64(v, r, off, 0, 16); }
DI void stc1(rsrc_t r, u32 off, u32 v) { __builtin_amdgcn_raw_buffer_store_b32(v, r, off, 0, 16); }
DI float asf(u32 u) { return __builtin_bit_cast(float, u); }
DI u32 asu(float f) { return __builtin_bit_cast(u32, f); }
DI u32 cvt2(float a, float b) { return __builtin_bit_cast(u32, __builtin_convertvector(f32x2{a, b}, bf16x2)); }
DI float blo(u32 u) { return asf(u << 16); }
DI float bhi(u32 u) { return asf(u & 0xFFFF0000u); }
DI float rbf(float f) { return blo(cvt2(f, f)); }
DI f32x4 mfma(u32x4 a, u32x4 b, f32x4 c) {
  return __builtin_amdgcn_mfma_f32_16x16x32_bf16(__builtin_bit_cast(bf16x8, a), __builtin_bit_cast(bf16x8, b), c, 0, 0, 0);
}
template <int CTRL> DI float dpp(float x) {
  return __builtin_bit_cast(float, __builtin_amdgcn_mov_dpp(__builtin_bit_cast(int, x), CTRL, 0xf, 0xf, true));
}
constexpr int XOR1 = 0xB1, XOR2 = 0x4E, ROR8 = 0x128, ROR4 = 0x124, HMIR = 0x141;
DI float tr32(float a, float b) { auto s = __builtin_amdgcn_permlane32_swap(asu(a), asu(b), false, false); return asf(s[0]) + asf(s[1]); }
DI float tr16(float a, float b) { auto s = __builtin_amdgcn_permlane16_swap(asu(a), asu(b), false, false); return asf(s[0]) + asf(s[1]); }
DI float max16(float x) { auto s = __builtin_amdgcn_permlane16_swap(asu(x), asu(x), false, false); return fmaxf(asf(s[0]), asf(s[1])); }
DI float max32(float x) { auto s = __builtin_amdgcn_permlane32_swap(asu(x), asu(x), false, false); return fmaxf(asf(s[0]), asf(s[1])); }
DI float sum8(float x) { x += dpp<XOR1>(x); x += dpp<XOR2>(x); x += dpp<HMIR>(x); return x; }
DI float max8(float x) { x = fmaxf(x, dpp<XOR1>(x)); x = fmaxf(x, dpp<XOR2>(x)); x = fmaxf(x, dpp<HMIR>(x)); return x; }
DI void lbar() { asm volatile("s_waitcnt lgkmcnt(0)" ::: "memory"); __builtin_amdgcn_s_barrier(); asm volatile("" ::: "memory"); }
DI void pin(u32x4 v) { asm volatile("" ::"v"(v)); }
DI void keep(u32& v) { asm volatile("" : "+v"(v)); }
DI void keep4(u32x4& v) { asm volatile("" : "+v"(v)); }
DI float ex2(float x) { return __builtin_amdgcn_exp2f(x); }
constexpr float LOG2E = 1.4426950408889634f;

DI u32 pk(float a, u32 tag) { return ((asu(a) + 0x800u) & 0xFFFFF000u) | tag; }
DI bool any_bad(u32 acc) { return __builtin_amdgcn_ballot_w64((acc & 0xFFFu) != 0) != 0; }
DI u32 or4(u32x4 v) { return (v[0] | v[1]) | (v[2] | v[3]); }
DI u32x4 onehot(u32 dup, const u32x4 mk4) { return u32x4{dup, dup, dup, dup} & mk4; }

extern __shared__ __attribute__((aligned(16))) char smem[];
template <class T> DI T lget(int off) { return *(const T*)(smem + off); }
template <class T> DI void lput(int off, T v) { *(T*)(smem + off) = v; }

DI u32x4 pk4(f32x4 v, u32 tag) { return u32x4{pk(v[0], tag), pk(v[1], tag), pk(v[2], tag), pk(v[3], tag)}; }
DI u32x4 xt(u32x4 v, u32 tag) { return v ^ u32x4{tag, tag, tag, tag}; }
DI f32x4 asf4(u32x4 v) { return __builtin_bit_cast(f32x4, v); }

__global__ __launch_bounds__(256, 2) void fused_kernel(const void* __restrict__ x, const void* __restrict__ wq, const void* __restrict__ kc,
                                                       void* ws, const void* __restrict__ vc, const void* __restrict__ w1,
                                                       const void* __restrict__ wo, const void* __restrict__ w2, u32* __restrict__ out) {
  STAMP(0);
  const u32 bid = blockIdx.x;
  const u32 xcd = bid & 7, jj = bid >> 3, p = xcd * 32 + jj;
  const u32 tid = threadIdx.x, lane = tid & 63, g = lane >> 4, n = lane & 15;
  const u32 w = __builtin_amdgcn_readfirstlane(tid >> 6);
  const u32 hkv = xcd >> 1;
  u32 vxs = tid * 16, vxa = (n >> 3) * 2048 + g * 16, vq = g * 65536 + n * 16;
  u32 vk = (4 * (n & 3) + ((n >> 2) & 1)) * 256 + (g + 4 * (n >> 3)) * 16;
  u32 vxf = (n & 1) * 2048 + (g & 1) * 4;
  u32 sxa = xcd * 256 + w * 64, sq = (xcd * 128 + w * 32) * 8192 + jj * 256;
  u32 kvb = (((xcd & 1) * 4 + hkv) * 32768 + 28672 + jj * 128 + w * 32) * 256u;
  keep(vxs); keep(vxa); keep(vq); keep(vk); keep(vxf);
  asm volatile("" : "+s"(sxa), "+s"(sq), "+s"(kvb));
  SB();
  asm volatile("" ::"s"(x), "s"(wq), "s"(kc), "s"(ws), "s"(vc), "s"(w1), "s"(wo), "s"(w2), "s"(out));
  SB();

  u32x4 WQ[8], K[8], V[8], W1[8], WO[8], W2[8];
  const rsrc_t rx = mk(x, 0, 4096);
  const u32x4 xs = __builtin_amdgcn_raw_buffer_load_b128(rx, vxs, 0, 0);
  const u32x4 xa = __builtin_amdgcn_raw_buffer_load_b128(rx, vxa, sxa, 0);
  SB();
  {
    const rsrc_t r = mk(wq, sq, 32 * 8192 - 7936);
#pragma unroll
    for (int j = 0; j < 8; ++j) WQ[j] = ldw(r, vq, j * 8192);
  }
  SB();
  const rsrc_t rw = mk(ws, 0, WS_USED);
  const u32 epoch = __builtin_amdgcn_raw_buffer_load_b32(rw, 0, 0, 16);
  const u32 xf = __builtin_amdgcn_raw_buffer_load_b32(rx, vxf, p * 8, 0);
  const rsrc_t rk = mk(kc, kvb, 8192), rv = mk(vc, kvb, 8192);
  auto LK = [&](int j) { SB(); K[j] = ldw(rk, vk + ((j >> 1) & 1) * 512 + (j >> 2) * 128, (j & 1) * 4096); SB(); };
  LK(0); LK(1);
  STAMP(1);
  const u32 cb1 = (jj + 1) & 31;
  const bool has1 = cb1 < 28;
  const rsrc_t r1 = mk(w1, has1 ? (xcd * 128 + w * 32) * 7168 + cb1 * 256 : 0, has1 ? 32 * 7168 - 6912 : 0);
  const u32 qd = (p >> 1) & 3, ch = p & 1;
  const rsrc_t ro = mk(wo, ((p >> 3) * 128 + 2 * qd) * 2048 + ch * 1024 + w * 256, 121 * 2048 + 256);
  const u32 j2 = (jj + 14) & 31;
  const bool has2 = j2 < 28;
  const u32 t2 = xcd * 28 + j2, k2 = t2 >> 1;
  const rsrc_t r2 = mk(w2, has2 ? k2 * 65536 + (t2 & 1) * 1024 + w * 256 : 0, has2 ? 31 * 2048 + 256 : 0);
  u32 vv = lane * 16, v1 = g * 57344 + n * 16, v2 = g * 16384 + n * 16;
  auto LV = [&](int j) { SB(); V[j] = ldw(rv, vv + (j >> 1) * 1024, (j & 1) * 4096); SB(); };
  auto L1 = [&](int j) { SB(); W1[j] = ldw(r1, v1, j * 7168); SB(); };
  auto LO = [&](int j) { SB(); WO[j] = ldw(ro, vq + (j & 1) * 2048, (j >> 1) * 16384); SB(); };
  auto L2 = [&](int j) { SB(); W2[j] = ldw(r2, v2 + (j & 1) * 2048, (j >> 1) * 4096); SB(); };
  u32 a_qst = lane < 16 ? QP_OFF + ((jj * 2 + (w >> 1)) * 8 + xcd) * 512 + (w & 1) * 256 + n * 16 : OOB;
  u32 a_qld = QP_OFF + ((hkv * 8 + 2 * w + (lane >> 5)) * 2 + (xcd & 1)) * 4096 + (lane & 31) * 16;
  u32 a_post = PO_OFF + p * 4096 + w * 1024 + (n & 7) * 128 + (n >> 3) * 64 + g * 16;
  u32 a_mlst = tid < 8 ? ML_OFF + (hkv * 8 + lane) * 1024 + (p & 63) * 16 : OOB;
  u32 a_hst = has1 && lane < 16 ? HP_OFF + (cb1 * 4 + w) * 2048 + (n >> 3) * 1024 + xcd * 128 + (n & 7) * 16 : OOB;
  u32 a_pold = PO_OFF + (hkv * 64 + 16 * w + (lane >> 3)) * 4096 + qd * 1024 + ((p >> 3) & 7) * 128 + (lane & 7) * 16;
  u32 a_mlld = ML_OFF + (p >> 3) * 1024 + ((w >> 1) * 32 + (((lane & 3) * 8) ^ ((w & 1) * 16)) + (lane >> 3)) * 16;
  const u32 cpart = (w * 32 + 2 * n + (g & 1)) * 32 + (g >> 1) * 16;
  u32 a_sst = SP_OFF + p * 4096 + cpart;
  u32 a_hld = has2 ? HP_OFF + k2 * 2048 + (n & 7) * 128 + (n >> 3) * 64 + g * 16 : OOB;
  u32 a_gst = has2 ? GP_OFF + t2 * 4096 + cpart : OOB;
  u32 a_fs = SP_OFF + (lane >> 1) * 8192 + p * 32 + (lane & 1) * 16;
  u32 l_rq = L_RQ + w * 256 + n * 16;
  u32 l_rh = L_RH + ((2 * ((n >> 2) & 1) + (n >> 3)) * 16 + 4 * w + (n & 3)) * 16;
  u32 l_qw = L_Q + (2 * w + (lane >> 5)) * 272 + (w >> 1) * 64 + n * 16 + (g & 1) * 8;
  u32 l_qr = L_Q + (n & 7) * 272 + ((n >> 2) & 1) * 64 + g * 16 + (n >> 3) * 64;
  u32 l_qs = L_Q + (n & 7) * 272 + ((n >> 2) & 1) * 64 + g * 16 + 64 - (n >> 3) * 64;
  u32 l_pw = L_PX + w * 128 + (lane & 3) * 32 + g * 8 + ((lane >> 2) & 1) * 4;
  u32 l_pr = L_PX + (n >> 3) * 256 + g * 32;
  u32x4 mk4, sel;
#pragma unroll
  for (u32 i = 0; i < 4; ++i) mk4[i] = (i == ((n & 7) >> 1)) ? ((n & 1) ? 0xFFFF0000u : 0x0000FFFFu) : 0u;
  sel = (n & 8) ? u32x4{0x05040c0cu, 0x07060c0cu, 0x01000c0cu, 0x03020c0cu} : u32x4{0x0c0c0100u, 0x0c0c0302u, 0x0c0c0504u, 0x0c0c0706u};
  keep(vv); keep(v1); keep(v2);
  keep(a_qst); keep(a_qld); keep(a_post); keep(a_mlst); keep(a_hst); keep(a_pold); keep(a_mlld);
  keep(a_sst); keep(a_hld); keep(a_gst); keep(a_fs); keep4(mk4); keep4(sel);
  keep(l_rq); keep(l_rh); keep(l_qw); keep(l_qr); keep(l_qs); keep(l_pw); keep(l_pr);
  SB();

  u32x4 AX[8];
  {
    pin(xs);
    LK(2); LK(3);
    const float pv = asf(__builtin_amdgcn_readfirstlane(xs[0]) << 16);
    float d[8];
#pragma unroll
    for (int k = 0; k < 4; ++k) { d[2 * k] = blo(xs[k]) - pv; d[2 * k + 1] = bhi(xs[k]) - pv; }
    const float s1 = ((d[0] + d[1]) + (d[2] + d[3])) + ((d[4] + d[5]) + (d[6] + d[7]));
    const float s2 = (d[0] * d[0] + d[1] * d[1] + d[2] * d[2] + d[3] * d[3]) + (d[4] * d[4] + d[5] * d[5] + d[6] * d[6] + d[7] * d[7]);
    float t = tr32(s1, s2);
    t = tr16(t, t);
    t += dpp<XOR1>(t); t += dpp<XOR2>(t); t += dpp<ROR4>(t); t += dpp<ROR8>(t);
    if ((lane & 31) == 0) lput<f32x2>(L_ST + w * 16 + (lane >> 5) * 8, f32x2{t, pv});
    lbar();
    const f32x4 a = lget<f32x4>(L_ST + (n >> 3) * 32), b = lget<f32x4>(L_ST + (n >> 3) * 32 + 16);
    const float da = a[0] * (1.0f / 512.0f), db = b[0] * (1.0f / 512.0f);
    const float ma = a[1] + da, mb = b[1] + db, dm = ma - mb;
    const float m2 = (a[2] - a[0] * da) + (b[2] - b[0] * db) + dm * dm * 256.0f;
    const u32 mv = cvt2((ma + mb) * 0.5f, fmaxf(m2, 0.0f) * (1.0f / 1024.0f));
    const float mr = blo(mv);
    const float rd = __builtin_amdgcn_rcpf(rbf(__builtin_amdgcn_sqrtf(rbf(bhi(mv) + 1e-5f))));
#pragma unroll
    for (int k = 0; k < 4; ++k) {
      const u32 c = cvt2(blo(xa[k]) - mr, bhi(xa[k]) - mr);
      const float e0 = blo(c) * rd, e1 = bhi(c) * rd;
      AX[2 * k] = onehot(cvt2(e0, e0), mk4);
      AX[2 * k + 1] = onehot(cvt2(e1, e1), mk4);
    }
#pragma unroll
    for (int j = 0; j < 8; ++j) keep4(AX[j]);
  }
  STAMP(2);
  u32 tag;
  {
    f32x4 acc = {0, 0, 0, 0};
#pragma unroll
    for (int j = 0; j < 8; ++j) {
      pin(WQ[j]);
      if (j >= 6) LK(j - 2);
      acc = mfma(AX[j], WQ[j], acc);
      SB();
    }
    STAMP(3);
    lput<f32x4>(L_RQ + vxs, acc);
    tag = (epoch + 1u) & 0xFFFu;
    keep(tag);
    lbar();
    const f32x4 v = (lget<f32x4>(l_rq) + lget<f32x4>(l_rq + 1024)) + (lget<f32x4>(l_rq + 2048) + lget<f32x4>(l_rq + 3072));
    stc4(rw, a_qst, pk4(v, tag));
  }
  SB();
  STAMP(4);
  pin(K[0]); LK(6);
  pin(K[1]); LK(7);
#pragma unroll
  for (int j = 2; j < 8; ++j) { pin(K[j]); LV(j - 2); }
  STAMP(5);
  u32x4 VT[2][4];
  {
    u32x4 Q[8];
#pragma unroll
    for (int i = 0; i < 8; ++i) Q[i] = ldc(rw, a_qld + i * 512, 0);
    SB();
    pin(V[0]); LV(6);
    pin(V[1]); LV(7);
#pragma unroll
    for (int j = 2; j < 6; ++j) { pin(V[j]); L1(j - 2); }
#pragma unroll
    for (int a = 0; a < 2; ++a)
#pragma unroll
      for (int i = 0; i < 4; ++i) { VT[a][i] = u32x4{V[a][i], V[2 + a][i], V[4 + a][i], 0}; keep4(VT[a][i]); }
    SB();
    f32x4 q;
    auto qred = [&]() __attribute__((always_inline)) -> u32 {
      u32x4 e = xt(Q[0], tag);
      u32 bad = or4(e);
      q = asf4(e);
#pragma unroll
      for (int i = 1; i < 8; ++i) { e = xt(Q[i], tag); bad |= or4(e); q += asf4(e); }
      asm volatile("" : "+v"(q));
      return bad;
    };
    const u32 bad0 = qred();
    STAMP(6);
    if (UNLIKELY(any_bad(bad0))) {
      u32 spin = 0, bad;
      do {
        asm volatile("" ::: "memory");
#pragma unroll
        for (int i = 0; i < 8; ++i) Q[i] = ldc(rw, a_qld + i * 512, 0);
        bad = qred();
      } while (any_bad(bad) && ++spin < SPIN_CAP);
    }
    lput<u32x2>(l_qw, u32x2{cvt2(q[0], q[1]), cvt2(q[2], q[3])});
  }
  L1(4);
  lbar();
  STAMP(7);
  {
    const u32x4 b00 = lget<u32x4>(l_qr), b01 = lget<u32x4>(l_qs), b10 = lget<u32x4>(l_qr + 128), b11 = lget<u32x4>(l_qs + 128);
    const f32x4 z = {0, 0, 0, 0};
    f32x4 c0 = mfma(K[0], b00, z), c1 = mfma(K[1], b01, z), c2 = mfma(K[2], b00, z), c3 = mfma(K[3], b01, z);
    c0 = mfma(K[4], b10, c0); c1 = mfma(K[5], b11, c1); c2 = mfma(K[6], b10, c2); c3 = mfma(K[7], b11, c3);
    L1(5);
    const bool same = (((lane >> 2) ^ lane) & 8) == 0;
    float sc[4];
#pragma unroll
    for (int r = 0; r < 4; ++r) {
      auto s = __builtin_amdgcn_permlane32_swap(asu(same ? c0[r] : c1[r]), asu(same ? c2[r] : c3[r]), false, false);
      sc[r] = asf(s[0]) + dpp<ROR8>(asf(s[1]));
    }
#pragma unroll
    for (int r = 0; r < 4; r += 2) {
      const u32 c = cvt2(sc[r], sc[r + 1]);
      const u32 e = cvt2(blo(c) * 0.08838834764831845f, bhi(c) * 0.08838834764831845f);
      sc[r] = blo(e); sc[r + 1] = bhi(e);
    }
    float m = fmaxf(fmaxf(sc[0], sc[1]), fmaxf(sc[2], sc[3]));
    m = fmaxf(m, dpp<ROR8>(m));
    m = max32(max16(m));
    const float mc = m * LOG2E;
    float e[4];
#pragma unroll
    for (int r = 0; r < 4; ++r) e[r] = ex2(__builtin_fmaf(sc[r], LOG2E, -mc));
    const u32 w01 = cvt2(e[0], e[1]), w23 = cvt2(e[2], e[3]);
    const u32 o01 = asu(dpp<ROR8>(asf(w01))), o23 = asu(dpp<ROR8>(asf(w23)));
    const u32x4 P0 = {__builtin_amdgcn_perm(o01, w01, sel[0]), __builtin_amdgcn_perm(o01, w01, sel[1]),
                      __builtin_amdgcn_perm(o23, w23, sel[0]), __builtin_amdgcn_perm(o23, w23, sel[1])};
    const u32x4 P1 = {__builtin_amdgcn_perm(o01, w01, sel[2]), __builtin_amdgcn_perm(o01, w01, sel[3]),
                      __builtin_amdgcn_perm(o23, w23, sel[2]), __builtin_amdgcn_perm(o23, w23, sel[3])};
    SB();
    pin(V[7]);
    L1(6);
    f32x4 o[4];
#pragma unroll
    for (int i = 0; i < 4; ++i) {
      VT[0][i][3] = V[6][i];
      VT[1][i][3] = V[7][i];
      o[i] = mfma(VT[0][i], P0, z);
    }
#pragma unroll
    for (int i = 0; i < 4; ++i) o[i] = mfma(VT[1][i], P1, o[i]);
    float l = (e[0] + e[1]) + (e[2] + e[3]);
    l += dpp<ROR8>(l);
    l = tr16(l, l);
    l = tr32(l, l);
    if ((lane & 31) < 8) lput<float>(L_ML + (lane & 7) * 32 + (lane >> 5) * 16 + w * 4, lane < 32 ? m : l);
#pragma unroll
    for (int i = 0; i < 4; ++i) lput<f32x4>(L_O + w * 4096 + i * 1024 + lane * 16, o[i]);
  }
  L1(7);
  lbar();
  {
    const f32x4 m4 = lget<f32x4>(L_ML + (n & 7) * 32), l4 = lget<f32x4>(L_ML + (n & 7) * 32 + 16);
    f32x4 ov[4];
#pragma unroll
    for (int i = 0; i < 4; ++i) ov[i] = lget<f32x4>(L_O + i * 4096 + vxs);
    const float M = fmaxf(fmaxf(m4[0], m4[1]), fmaxf(m4[2], m4[3]));
    float f[4];
#pragma unroll
    for (int i = 0; i < 4; ++i) f[i] = ex2((m4[i] - M) * LOG2E);
    stc4(rw, a_post, pk4(f[0] * ov[0] + f[1] * ov[1] + f[2] * ov[2] + f[3] * ov[3], tag));
    const float L = f[0] * l4[0] + f[1] * l4[1] + f[2] * l4[2] + f[3] * l4[3];
    stc4(rw, a_mlst, u32x4{asu(M) | tag, pk(L, tag), tag, tag});
  }
  SB();
  STAMP(8);
  LO(0);
  {
    f32x4 acc = {0, 0, 0, 0};
#pragma unroll
    for (int j = 0; j < 8; ++j) {
      pin(W1[j]);
      if (j >= 3) LO(j - 2);
      acc = mfma(AX[j], W1[j], acc);
      SB();
    }
    STAMP(9);
    lput<f32x4>(L_RH + vxs, acc);
    lbar();
    const f32x4 v = (lget<f32x4>(l_rh) + lget<f32x4>(l_rh + 1024)) + (lget<f32x4>(l_rh + 2048) + lget<f32x4>(l_rh + 3072));
    stc4(rw, a_hst, pk4(v, tag));
  }
  SB();
  STAMP(10);
  u32x4 H[2], H2[2];
  {
    pin(WO[0]); LO(6);
    pin(WO[1]); LO(7);
#pragma unroll
    for (int j = 0; j < POPOS; ++j) { pin(WO[2 + j]); L2(j); }
    u32x4 MA = ldc(rw, a_mlld, 0), PA0 = ldc(rw, a_pold, 0), PA1 = ldc(rw, a_pold, 8 * 4096);
    H[0] = ldc(rw, a_hld, 0);
    H[1] = ldc(rw, a_hld + 1024, 0);
    SB();
#pragma unroll
    for (int j = POPOS; j < 8; ++j) { if (j + 2 - POBURST < 8 && j + 2 - POBURST >= POPOS + 2) pin(WO[j + 2 - POBURST]); L2(j); }
    float y;
    auto ored = [&]() __attribute__((always_inline)) -> u32 {
      const u32x4 ml = xt(MA, tag);
      const float m = asf(ml[0]);
      float M = fmaxf(m, dpp<XOR1>(m));
      M = fmaxf(M, dpp<XOR2>(M));
      M = fmaxf(M, dpp<ROR8>(M));
      M = max32(max16(M));
      const float e = ex2((m - M) * LOG2E);
      float L = e * asf(ml[1]);
      L += dpp<XOR1>(L); L += dpp<XOR2>(L); L += dpp<ROR8>(L);
      L = tr16(L, L);
      L = tr32(L, L);
      const float e0 = dpp<0x00>(e), e1 = dpp<0x55>(e);
      const u32x4 t0 = xt(PA0, tag), t1 = xt(PA1, tag);
      const f32x4 o = e0 * asf4(t0) + e1 * asf4(t1);
      y = tr16(tr32(o[0], o[2]), tr32(o[1], o[3]));
      y += dpp<ROR8>(y);
      y *= __builtin_amdgcn_rcpf(L);
      asm volatile("" : "+v"(y));
      return or4(ml) | or4(t0) | or4(t1);
    };
    const u32 bad0 = ored();
    STAMP(11);
    if (UNLIKELY(any_bad(bad0))) {
      u32 spin = 0, bad;
      do {
        asm volatile("" ::: "memory");
        MA = ldc(rw, a_mlld, 0); PA0 = ldc(rw, a_pold, 0); PA1 = ldc(rw, a_pold, 8 * 4096);
        bad = ored();
      } while (any_bad(bad) && ++spin < SPIN_CAP);
    }
    lput<float>(l_pw, y);
    SB();
    H2[0] = ldc(rw, a_hld, 0);
    H2[1] = ldc(rw, a_hld + 1024, 0);
    SB();
    lbar();
    const f32x4 oa = lget<f32x4>(l_pr) + lget<f32x4>(l_pr + 128), ob = lget<f32x4>(l_pr + 16) + lget<f32x4>(l_pr + 144);
    f32x4 acc = {0, 0, 0, 0};
#pragma unroll
    for (int j = 0; j < 8; ++j) {
      const float t = j < 4 ? oa[j & 3] : ob[j & 3];
      acc = mfma(onehot(cvt2(t, t), mk4), WO[j], acc);
    }
    stc4(rw, a_sst, pk4(acc, tag));
  }
  SB();
  STAMP(12);
  if (has2) {
    u32 hd[8];
    auto hred = [&](const u32x4 (&X)[2]) __attribute__((always_inline)) -> u32 {
      const u32x4 t0 = xt(X[0], tag), t1 = xt(X[1], tag);
#pragma unroll
      for (int j = 0; j < 8; ++j) { const float t = fmaxf(sum8(asf(j < 4 ? t0[j & 3] : t1[j & 3])), 0.0f); hd[j] = cvt2(t, t); keep(hd[j]); }
      return or4(t0) | or4(t1);
    };
    if (UNLIKELY(any_bad(hred(H)))) {
      asm volatile("" ::: "memory");
      if (any_bad(hred(H2))) {
        u32 spin = 0, bad;
        do {
          asm volatile("" ::: "memory");
          H[0] = ldc(rw, a_hld, 0);
          H[1] = ldc(rw, a_hld + 1024, 0);
          bad = hred(H);
        } while (any_bad(bad) && ++spin < SPIN_CAP);
      }
    }
    STAMP(13);
    u32x4 AH[8];
#pragma unroll
    for (int j = 0; j < 8; ++j) { AH[j] = onehot(hd[j], mk4); keep4(AH[j]); }
    SB();
    f32x4 acc = {0, 0, 0, 0};
#pragma unroll
    for (int j = 0; j < 8; ++j) acc = mfma(AH[j], W2[j], acc);
    stc4(rw, a_gst, pk4(acc, tag));
  }
  SB();
  STAMP(14);
  if (w != 0) { pin(H2[0]); pin(H2[1]); }
  if (w == 0) {
    const u32 gt = lane < 32 ? tag : 0u;
    const u32 a_fg = lane < 32 ? a_fs : OOB;
    u32x4 S[4], G[4];
    f32x4 s, gs;
    auto ldS = [&]() __attribute__((always_inline)) {
#pragma unroll
      for (int i = 0; i < 4; ++i) S[i] = ldc(rw, a_fs, i * 32 * 8192);
    };
    auto ldG = [&]() __attribute__((always_inline)) {
#pragma unroll
      for (int i = 0; i < 4; ++i) G[i] = ldc(rw, i < 3 ? a_fs : a_fg, (4 + i) * 32 * 8192);
    };
    auto red = [&](const u32x4 (&X)[4], u32 lt, f32x4& r) __attribute__((always_inline)) -> u32 {
      const u32x4 t0 = xt(X[0], tag), t1 = xt(X[1], tag), t2 = xt(X[2], tag), t3 = xt(X[3], lt);
      r = (asf4(t0) + asf4(t1)) + (asf4(t2) + asf4(t3));
      asm volatile("" : "+v"(r));
      return or4(t0) | or4(t1) | or4(t2) | or4(t3);
    };
    ldS(); ldG();
    SB();
    u32 spin = 0;
    if (UNLIKELY(any_bad(red(S, tag, s)))) {
      u32 bad;
      do {
        asm volatile("" ::: "memory");
        ldS();
        bad = red(S, tag, s);
      } while (any_bad(bad) && ++spin < SPIN_CAP);
    }
    while (any_bad(red(G, gt, gs)) && ++spin < SPIN_CAP) {
      asm volatile("" ::: "memory");
      ldG();
    }
    pin(H2[0]); pin(H2[1]);
    float u[4];
#pragma unroll
    for (int r = 0; r < 4; ++r) u[r] = tr32(s[r], gs[r]);
    float y0 = tr16(u[0], u[2]), y1 = tr16(u[1], u[3]);
    y0 += dpp<XOR2>(y0); y1 += dpp<XOR2>(y1);
    y0 += dpp<ROR4>(y0); y1 += dpp<ROR4>(y1);
    y0 += dpp<ROR8>(y0); y1 += dpp<ROR8>(y1);
    const u32 yb = cvt2(y0, y1);
    auto sw = __builtin_amdgcn_permlane32_swap(yb, yb, false, false);
    const float g0 = blo(sw[1]), g1 = bhi(sw[1]);
    const u32 sg = cvt2(g0 * __builtin_amdgcn_rcpf(1.0f + ex2(-LOG2E * g0)), g1 * __builtin_amdgcn_rcpf(1.0f + ex2(-LOG2E * g1)));
    const u32 pr = cvt2(blo(sw[0]) * blo(sg), bhi(sw[0]) * bhi(sg));
    const u32 res = cvt2(blo(xf) + blo(pr), bhi(xf) + bhi(pr));
    if ((lane & 46) == 0) out[(n & 1) * 512 + p * 2 + (g & 1)] = res;
    if (p == 0 && lane == 0) __hip_atomic_store((u32*)ws, tag, __ATOMIC_RELAXED, __HIP_MEMORY_SCOPE_AGENT);
  }
}

extern "C" void kernel_launch(void* const* d_in, const int* in_sizes, int n_in, void* d_out, int out_size, void* d_ws,
                              size_t ws_size, hipStream_t stream) {
  (void)in_sizes; (void)n_in; (void)out_size; (void)ws_size;
  (void)hipFuncSetAttribute((const void*)fused_kernel, hipFuncAttributeMaxDynamicSharedMemorySize, LDS_BYTES);
  fused_kernel<<<256, 256, LDS_BYTES, stream>>>(d_in[0], d_in[3], d_in[1], d_ws, d_in[2], d_in[6], d_in[4], d_in[7], (u32*)d_out);
}
```

```cpp
#include <hip/hip_runtime.h>
#include <hip/hip_bf16.h>
#include <stdint.h>

typedef unsigned int u32;
typedef unsigned long long u64;
typedef u32 u32x4 __attribute__((ext_vector_type(4)));
typedef u32 u32x2 __attribute__((ext_vector_type(2)));
typedef float f32x4 __attribute__((ext_vector_type(4)));
typedef float f32x2 __attribute__((ext_vector_type(2)));
typedef short bf16x8 __attribute__((ext_vector_type(8)));
typedef __bf16 bf16x2 __attribute__((ext_vector_type(2)));
typedef __amdgpu_buffer_rsrc_t rsrc_t;
#define DI __device__ __forceinline__
#define SB() __builtin_amdgcn_sched_barrier(0)
#define UNLIKELY(x) __builtin_expect(!!(x), 0)

constexpr u32 QP_OFF = 4096;
constexpr u32 HP_OFF = QP_OFF + 32 * 2 * 8 * 512;
constexpr u32 PO_OFF = HP_OFF + 112 * 2048;
constexpr u32 ML_OFF = PO_OFF + 256 * 4096;
constexpr u32 SP_OFF = ML_OFF + 32 * 1024;
constexpr u32 GP_OFF = SP_OFF + 128 * 8192;
constexpr u32 WS_USED = GP_OFF + 112 * 8192;
#define POPOS 2
#define POBURST 8
constexpr u32 OOB = 0x80000000u;
constexpr u32 SPIN_CAP = 1u << 16;
#define STAMP(i) do { if ((i) == 0 || (i) == 0) { SB(); stamp[(i) ? 1 : 0] = __builtin_readsteadycounter(); SB(); } } while (0)

constexpr int L_ST = 0;
constexpr int L_PX = 256;
constexpr int L_RQ = 1024;
constexpr int L_RH = L_RQ + 4096;
constexpr int L_Q = L_RH + 4096;
constexpr int L_ML = L_Q + 2304;
constexpr int L_O = L_ML + 256;
constexpr int L_END = L_O + 4 * 4096;
constexpr int LDS_BYTES = 84 * 1024;
static_assert(L_END <= LDS_BYTES, "lds");

DI rsrc_t mk(const void* p, u32 off, u32 bytes) { return __builtin_amdgcn_make_buffer_rsrc((char*)p + off, 0, bytes, 0x00020000); }
DI u32x4 ldw(rsrc_t r, u32 v, u32 s) { return __builtin_amdgcn_raw_buffer_load_b128(r, v, s, 2); }
DI u32x4 ldc(rsrc_t r, u32 v, u32 s) { return __builtin_amdgcn_raw_buffer_load_b128(r, v, s, 16); }
DI void stc4(rsrc_t r, u32 off, u32x4 v) { __builtin_amdgcn_raw_buffer_store_b128(v, r, off, 0, 16); }
DI void stc2(rsrc_t r, u32 off, u32x2 v) { __builtin_amdgcn_raw_buffer_store_b64(v, r, off, 0, 16); }
DI void stc1(rsrc_t r, u32 off, u32 v) { __builtin_amdgcn_raw_buffer_store_b32(v, r, off, 0, 16); }
DI float asf(u32 u) { return __builtin_bit_cast(float, u); }
DI u32 asu(float f) { return __builtin_bit_cast(u32, f); }
DI u32 cvt2(float a, float b) { return __builtin_bit_cast(u32, __builtin_convertvector(f32x2{a, b}, bf16x2)); }
DI float blo(u32 u) { return asf(u << 16); }
DI float bhi(u32 u) { return asf(u & 0xFFFF0000u); }
DI float rbf(float f) { return blo(cvt2(f, f)); }
DI f32x4 mfma(u32x4 a, u32x4 b, f32x4 c) {
  return __builtin_amdgcn_mfma_f32_16x16x32_bf16(__builtin_bit_cast(bf16x8, a), __builtin_bit_cast(bf16x8, b), c, 0, 0, 0);
}
template <int CTRL> DI float dpp(float x) {
  return __builtin_bit_cast(float, __builtin_amdgcn_mov_dpp(__builtin_bit_cast(int, x), CTRL, 0xf, 0xf, true));
}
constexpr int XOR1 = 0xB1, XOR2 = 0x4E, ROR8 = 0x128, ROR4 = 0x124, HMIR = 0x141;
DI float tr32(float a, float b) { auto s = __builtin_amdgcn_permlane32_swap(asu(a), asu(b), false, false); return asf(s[0]) + asf(s[1]); }
DI float tr16(float a, float b) { auto s = __builtin_amdgcn_permlane16_swap(asu(a), asu(b), false, false); return asf(s[0]) + asf(s[1]); }
DI float max16(float x) { auto s = __builtin_amdgcn_permlane16_swap(asu(x), asu(x), false, false); return fmaxf(asf(s[0]), asf(s[1])); }
DI float max32(float x) { auto s = __builtin_amdgcn_permlane32_swap(asu(x), asu(x), false, false); return fmaxf(asf(s[0]), asf(s[1])); }
DI float sum8(float x) { x += dpp<XOR1>(x); x += dpp<XOR2>(x); x += dpp<HMIR>(x); return x; }
DI float max8(float x) { x = fmaxf(x, dpp<XOR1>(x)); x = fmaxf(x, dpp<XOR2>(x)); x = fmaxf(x, dpp<HMIR>(x)); return x; }
DI void lbar() { asm volatile("s_waitcnt lgkmcnt(0)" ::: "memory"); __builtin_amdgcn_s_barrier(); asm volatile("" ::: "memory"); }
DI void pin(u32x4 v) { asm volatile("" ::"v"(v)); }
DI void keep(u32& v) { asm volatile("" : "+v"(v)); }
DI void keep4(u32x4& v) { asm volatile("" : "+v"(v)); }
DI float ex2(float x) { return __builtin_amdgcn_exp2f(x); }
constexpr float LOG2E = 1.4426950408889634f;

DI u32 pk(float a, u32 tag) { return ((asu(a) + 0x800u) & 0xFFFFF000u) | tag; }
DI bool any_bad(u32 acc) { return __builtin_amdgcn_ballot_w64((acc & 0xFFFu) != 0) != 0; }
DI u32 or4(u32x4 v) { return (v[0] | v[1]) | (v[2] | v[3]); }
DI u32x4 onehot(u32 dup, const u32x4 mk4) { return u32x4{dup, dup, dup, dup} & mk4; }

extern __shared__ __attribute__((aligned(16))) char smem[];
template <class T> DI T lget(int off) { return *(const T*)(smem + off); }
template <class T> DI void lput(int off, T v) { *(T*)(smem + off) = v; }

DI u32x4 pk4(f32x4 v, u32 tag) { return u32x4{pk(v[0], tag), pk(v[1], tag), pk(v[2], tag), pk(v[3], tag)}; }
DI u32x4 xt(u32x4 v, u32 tag) { return v ^ u32x4{tag, tag, tag, tag}; }
DI f32x4 asf4(u32x4 v) { return __builtin_bit_cast(f32x4, v); }

__global__ __launch_bounds__(256, 2) void fused_kernel(const void* __restrict__ x, const void* __restrict__ wq, const void* __restrict__ kc,
                                                       void* ws, const void* __restrict__ vc, const void* __restrict__ w1,
                                                       const void* __restrict__ wo, const void* __restrict__ w2, u32* __restrict__ out) {
  u64 stamp[2] = {0, 0};
  STAMP(0);
  for (int i = 0; i < 100000 && __builtin_readsteadycounter() - stamp[0] < 1500; ++i) __builtin_amdgcn_s_sleep(1);
  const u32 bid = blockIdx.x;
  const u32 xcd = bid & 7, jj = bid >> 3, p = xcd * 32 + jj;
  const u32 tid = threadIdx.x, lane = tid & 63, g = lane >> 4, n = lane & 15;
  const u32 w = __builtin_amdgcn_readfirstlane(tid >> 6);
  const u32 hkv = xcd >> 1;
  u32 vxs = tid * 16, vxa = (n >> 3) * 2048 + g * 16, vq = g * 65536 + n * 16;
  u32 vk = (4 * (n & 3) + ((n >> 2) & 1)) * 256 + (g + 4 * (n >> 3)) * 16;
  u32 vxf = (n & 1) * 2048 + (g & 1) * 4;
  u32 sxa = xcd * 256 + w * 64, sq = (xcd * 128 + w * 32) * 8192 + jj * 256;
  u32 kvb = (((xcd & 1) * 4 + hkv) * 32768 + 28672 + jj * 128 + w * 32) * 256u;
  keep(vxs); keep(vxa); keep(vq); keep(vk); keep(vxf);
  asm volatile("" : "+s"(sxa), "+s"(sq), "+s"(kvb));
  SB();
  asm volatile("" ::"s"(x), "s"(wq), "s"(kc), "s"(ws), "s"(vc), "s"(w1), "s"(wo), "s"(w2), "s"(out));
  SB();

  u32x4 WQ[8], K[8], V[8], W1[8], WO[8], W2[8];
  const rsrc_t rx = mk(x, 0, 4096);
  const u32x4 xs = __builtin_amdgcn_raw_buffer_load_b128(rx, vxs, 0, 0);
  const u32x4 xa = __builtin_amdgcn_raw_buffer_load_b128(rx, vxa, sxa, 0);
  SB();
  {
    const rsrc_t r = mk(wq, sq, 32 * 8192 - 7936);
#pragma unroll
    for (int j = 0; j < 8; ++j) WQ[j] = ldw(r, vq, j * 8192);
  }
  SB();
  const rsrc_t rw = mk(ws, 0, WS_USED);
  const u32 epoch = __builtin_amdgcn_raw_buffer_load_b32(rw, 0, 0, 16);
  const u32 xf = __builtin_amdgcn_raw_buffer_load_b32(rx, vxf, p * 8, 0);
  const rsrc_t rk = mk(kc, kvb, 8192), rv = mk(vc, kvb, 8192);
  auto LK = [&](int j) { SB(); K[j] = ldw(rk, vk + ((j >> 1) & 1) * 512 + (j >> 2) * 128, (j & 1) * 4096); SB(); };
  LK(0); LK(1);
  STAMP(1);
  const u32 cb1 = (jj + 1) & 31;
  const bool has1 = cb1 < 28;
  const rsrc_t r1 = mk(w1, has1 ? (xcd * 128 + w * 32) * 7168 + cb1 * 256 : 0, has1 ? 32 * 7168 - 6912 : 0);
  const u32 qd = (p >> 1) & 3, ch = p & 1;
  const rsrc_t ro = mk(wo, ((p >> 3) * 128 + 2 * qd) * 2048 + ch * 1024 + w * 256, 121 * 2048 + 256);
  const u32 j2 = (jj + 14) & 31;
  const bool has2 = j2 < 28;
  const u32 t2 = xcd * 28 + j2, k2 = t2 >> 1;
  const rsrc_t r2 = mk(w2, has2 ? k2 * 65536 + (t2 & 1) * 1024 + w * 256 : 0, has2 ? 31 * 2048 + 256 : 0);
  u32 vv = lane * 16, v1 = g * 57344 + n * 16, v2 = g * 16384 + n * 16;
  auto LV = [&](int j) { SB(); V[j] = ldw(rv, vv + (j >> 1) * 1024, (j & 1) * 4096); SB(); };
  auto L1 = [&](int j) { SB(); W1[j] = ldw(r1, v1, j * 7168); SB(); };
  auto LO = [&](int j) { SB(); WO[j] = ldw(ro, vq + (j & 1) * 2048, (j >> 1) * 16384); SB(); };
  auto L2 = [&](int j) { SB(); W2[j] = ldw(r2, v2 + (j & 1) * 2048, (j >> 1) * 4096); SB(); };
  u32 a_qst = lane < 16 ? QP_OFF + ((jj * 2 + (w >> 1)) * 8 + xcd) * 512 + (w & 1) * 256 + n * 16 : OOB;
  u32 a_qld = QP_OFF + ((hkv * 8 + 2 * w + (lane >> 5)) * 2 + (xcd & 1)) * 4096 + (lane & 31) * 16;
  u32 a_post = PO_OFF + p * 4096 + w * 1024 + (n & 7) * 128 + (n >> 3) * 64 + g * 16;
  u32 a_mlst = tid < 8 ? ML_OFF + (hkv * 8 + lane) * 1024 + (p & 63) * 16 : OOB;
  u32 a_hst = has1 && lane < 16 ? HP_OFF + (cb1 * 4 + w) * 2048 + (n >> 3) * 1024 + xcd * 128 + (n & 7) * 16 : OOB;
  u32 a_pold = PO_OFF + (hkv * 64 + 16 * w + (lane >> 3)) * 4096 + qd * 1024 + ((p >> 3) & 7) * 128 + (lane & 7) * 16;
  u32 a_mlld = ML_OFF + (p >> 3) * 1024 + ((w >> 1) * 32 + (((lane & 3) * 8) ^ ((w & 1) * 16)) + (lane >> 3)) * 16;
  const u32 cpart = (w * 32 + 2 * n + (g & 1)) * 32 + (g >> 1) * 16;
  u32 a_sst = SP_OFF + p * 4096 + cpart;
  u32 a_hld = has2 ? HP_OFF + k2 * 2048 + (n & 7) * 128 + (n >> 3) * 64 + g * 16 : OOB;
  u32 a_gst = has2 ? GP_OFF + t2 * 4096 + cpart : OOB;
  u32 a_fs = SP_OFF + (lane >> 1) * 8192 + p * 32 + (lane & 1) * 16;
  u32 l_rq = L_RQ + w * 256 + n * 16;
  u32 l_rh = L_RH + ((2 * ((n >> 2) & 1) + (n >> 3)) * 16 + 4 * w + (n & 3)) * 16;
  u32 l_qw = L_Q + (2 * w + (lane >> 5)) * 272 + (w >> 1) * 64 + n * 16 + (g & 1) * 8;
  u32 l_qr = L_Q + (n & 7) * 272 + ((n >> 2) & 1) * 64 + g * 16 + (n >> 3) * 64;
  u32 l_qs = L_Q + (n & 7) * 272 + ((n >> 2) & 1) * 64 + g * 16 + 64 - (n >> 3) * 64;
  u32 l_pw = L_PX + w * 128 + (lane & 3) * 32 + g * 8 + ((lane >> 2) & 1) * 4;
  u32 l_pr = L_PX + (n >> 3) * 256 + g * 32;
  u32x4 mk4, sel;
#pragma unroll
  for (u32 i = 0; i < 4; ++i) mk4[i] = (i == ((n & 7) >> 1)) ? ((n & 1) ? 0xFFFF0000u : 0x0000FFFFu) : 0u;
  sel = (n & 8) ? u32x4{0x05040c0cu, 0x07060c0cu, 0x01000c0cu, 0x03020c0cu} : u32x4{0x0c0c0100u, 0x0c0c0302u, 0x0c0c0504u, 0x0c0c0706u};
  keep(vv); keep(v1); keep(v2);
  keep(a_qst); keep(a_qld); keep(a_post); keep(a_mlst); keep(a_hst); keep(a_pold); keep(a_mlld);
  keep(a_sst); keep(a_hld); keep(a_gst); keep(a_fs); keep4(mk4); keep4(sel);
  keep(l_rq); keep(l_rh); keep(l_qw); keep(l_qr); keep(l_qs); keep(l_pw); keep(l_pr);
  SB();

  u32x4 AX[8];
  {
    pin(xs);
    LK(2); LK(3);
    const float pv = asf(__builtin_amdgcn_readfirstlane(xs[0]) << 16);
    float d[8];
#pragma unroll
    for (int k = 0; k < 4; ++k) { d[2 * k] = blo(xs[k]) - pv; d[2 * k + 1] = bhi(xs[k]) - pv; }
    const float s1 = ((d[0] + d[1]) + (d[2] + d[3])) + ((d[4] + d[5]) + (d[6] + d[7]));
    const float s2 = (d[0] * d[0] + d[1] * d[1] + d[2] * d[2] + d[3] * d[3]) + (d[4] * d[4] + d[5] * d[5] + d[6] * d[6] + d[7] * d[7]);
    float t = tr32(s1, s2);
    t = tr16(t, t);
    t += dpp<XOR1>(t); t += dpp<XOR2>(t); t += dpp<ROR4>(t); t += dpp<ROR8>(t);
    if ((lane & 31) == 0) lput<f32x2>(L_ST + w * 16 + (lane >> 5) * 8, f32x2{t, pv});
    lbar();
    const f32x4 a = lget<f32x4>(L_ST + (n >> 3) * 32), b = lget<f32x4>(L_ST + (n >> 3) * 32 + 16);
    const float da = a[0] * (1.0f / 512.0f), db = b[0] * (1.0f / 512.0f);
    const float ma = a[1] + da, mb = b[1] + db, dm = ma - mb;
    const float m2 = (a[2] - a[0] * da) + (b[2] - b[0] * db) + dm * dm * 256.0f;
    const u32 mv = cvt2((ma + mb) * 0.5f, fmaxf(m2, 0.0f) * (1.0f / 1024.0f));
    const float mr = blo(mv);
    const float rd = __builtin_amdgcn_rcpf(rbf(__builtin_amdgcn_sqrtf(rbf(bhi(mv) + 1e-5f))));
#pragma unroll
    for (int k = 0; k < 4; ++k) {
      const u32 c = cvt2(blo(xa[k]) - mr, bhi(xa[k]) - mr);
      const float e0 = blo(c) * rd, e1 = bhi(c) * rd;
      AX[2 * k] = onehot(cvt2(e0, e0), mk4);
      AX[2 * k + 1] = onehot(cvt2(e1, e1), mk4);
    }
#pragma unroll
    for (int j = 0; j < 8; ++j) keep4(AX[j]);
  }
  STAMP(2);
  u32 tag;
  {
    f32x4 acc = {0, 0, 0, 0};
#pragma unroll
    for (int j = 0; j < 8; ++j) {
      pin(WQ[j]);
      if (j >= 6) LK(j - 2);
      acc = mfma(AX[j], WQ[j], acc);
      SB();
    }
    STAMP(3);
    lput<f32x4>(L_RQ + vxs, acc);
    tag = (epoch + 1u) & 0xFFFu;
    keep(tag);
    lbar();
    const f32x4 v = (lget<f32x4>(l_rq) + lget<f32x4>(l_rq + 1024)) + (lget<f32x4>(l_rq + 2048) + lget<f32x4>(l_rq + 3072));
    stc4(rw, a_qst, pk4(v, tag));
  }
  SB();
  STAMP(4);
  pin(K[0]); LK(6);
  pin(K[1]); LK(7);
#pragma unroll
  for (int j = 2; j < 8; ++j) { pin(K[j]); LV(j - 2); }
  STAMP(5);
  u32x4 VT[2][4];
  {
    u32x4 Q[8];
#pragma unroll
    for (int i = 0; i < 8; ++i) Q[i] = ldc(rw, a_qld + i * 512, 0);
    SB();
    pin(V[0]); LV(6);
    pin(V[1]); LV(7);
#pragma unroll
    for (int j = 2; j < 6; ++j) { pin(V[j]); L1(j - 2); }
#pragma unroll
    for (int a = 0; a < 2; ++a)
#pragma unroll
      for (int i = 0; i < 4; ++i) { VT[a][i] = u32x4{V[a][i], V[2 + a][i], V[4 + a][i], 0}; keep4(VT[a][i]); }
    SB();
    f32x4 q;
    auto qred = [&]() __attribute__((always_inline)) -> u32 {
      u32x4 e = xt(Q[0], tag);
      u32 bad = or4(e);
      q = asf4(e);
#pragma unroll
      for (int i = 1; i < 8; ++i) { e = xt(Q[i], tag); bad |= or4(e); q += asf4(e); }
      asm volatile("" : "+v"(q));
      return bad;
    };
    const u32 bad0 = qred();
    STAMP(6);
    if (UNLIKELY(any_bad(bad0))) {
      u32 spin = 0, bad;
      do {
        asm volatile("" ::: "memory");
#pragma unroll
        for (int i = 0; i < 8; ++i) Q[i] = ldc(rw, a_qld + i * 512, 0);
        bad = qred();
      } while (any_bad(bad) && ++spin < SPIN_CAP);
    }
    lput<u32x2>(l_qw, u32x2{cvt2(q[0], q[1]), cvt2(q[2], q[3])});
  }
  L1(4);
  lbar();
  STAMP(7);
  {
    const u32x4 b00 = lget<u32x4>(l_qr), b01 = lget<u32x4>(l_qs), b10 = lget<u32x4>(l_qr + 128), b11 = lget<u32x4>(l_qs + 128);
    const f32x4 z = {0, 0, 0, 0};
    f32x4 c0 = mfma(K[0], b00, z), c1 = mfma(K[1], b01, z), c2 = mfma(K[2], b00, z), c3 = mfma(K[3], b01, z);
    c0 = mfma(K[4], b10, c0); c1 = mfma(K[5], b11, c1); c2 = mfma(K[6], b10, c2); c3 = mfma(K[7], b11, c3);
    L1(5);
    const bool same = (((lane >> 2) ^ lane) & 8) == 0;
    float sc[4];
#pragma unroll
    for (int r = 0; r < 4; ++r) {
      auto s = __builtin_amdgcn_permlane32_swap(asu(same ? c0[r] : c1[r]), asu(same ? c2[r] : c3[r]), false, false);
      sc[r] = asf(s[0]) + dpp<ROR8>(asf(s[1]));
    }
#pragma unroll
    for (int r = 0; r < 4; r += 2) {
      const u32 c = cvt2(sc[r], sc[r + 1]);
      const u32 e = cvt2(blo(c) * 0.08838834764831845f, bhi(c) * 0.08838834764831845f);
      sc[r] = blo(e); sc[r + 1] = bhi(e);
    }
    float m = fmaxf(fmaxf(sc[0], sc[1]), fmaxf(sc[2], sc[3]));
    m = fmaxf(m, dpp<ROR8>(m));
    m = max32(max16(m));
    const float mc = m * LOG2E;
    float e[4];
#pragma unroll
    for (int r = 0; r < 4; ++r) e[r] = ex2(__builtin_fmaf(sc[r], LOG2E, -mc));
    const u32 w01 = cvt2(e[0], e[1]), w23 = cvt2(e[2], e[3]);
    const u32 o01 = asu(dpp<ROR8>(asf(w01))), o23 = asu(dpp<ROR8>(asf(w23)));
    const u32x4 P0 = {__builtin_amdgcn_perm(o01, w01, sel[0]), __builtin_amdgcn_perm(o01, w01, sel[1]),
                      __builtin_amdgcn_perm(o23, w23, sel[0]), __builtin_amdgcn_perm(o23, w23, sel[1])};
    const u32x4 P1 = {__builtin_amdgcn_perm(o01, w01, sel[2]), __builtin_amdgcn_perm(o01, w01, sel[3]),
                      __builtin_amdgcn_perm(o23, w23, sel[2]), __builtin_amdgcn_perm(o23, w23, sel[3])};
    SB();
    pin(V[7]);
    L1(6);
    f32x4 o[4];
#pragma unroll
    for (int i = 0; i < 4; ++i) {
      VT[0][i][3] = V[6][i];
      VT[1][i][3] = V[7][i];
      o[i] = mfma(VT[0][i], P0, z);
    }
#pragma unroll
    for (int i = 0; i < 4; ++i) o[i] = mfma(VT[1][i], P1, o[i]);
    float l = (e[0] + e[1]) + (e[2] + e[3]);
    l += dpp<ROR8>(l);
    l = tr16(l, l);
    l = tr32(l, l);
    if ((lane & 31) < 8) lput<float>(L_ML + (lane & 7) * 32 + (lane >> 5) * 16 + w * 4, lane < 32 ? m : l);
#pragma unroll
    for (int i = 0; i < 4; ++i) lput<f32x4>(L_O + w * 4096 + i * 1024 + lane * 16, o[i]);
  }
  L1(7);
  lbar();
  {
    const f32x4 m4 = lget<f32x4>(L_ML + (n & 7) * 32), l4 = lget<f32x4>(L_ML + (n & 7) * 32 + 16);
    f32x4 ov[4];
#pragma unroll
    for (int i = 0; i < 4; ++i) ov[i] = lget<f32x4>(L_O + i * 4096 + vxs);
    const float M = fmaxf(fmaxf(m4[0], m4[1]), fmaxf(m4[2], m4[3]));
    float f[4];
#pragma unroll
    for (int i = 0; i < 4; ++i) f[i] = ex2((m4[i] - M) * LOG2E);
    stc4(rw, a_post, pk4(f[0] * ov[0] + f[1] * ov[1] + f[2] * ov[2] + f[3] * ov[3], tag));
    const float L = f[0] * l4[0] + f[1] * l4[1] + f[2] * l4[2] + f[3] * l4[3];
    stc4(rw, a_mlst, u32x4{asu(M) | tag, pk(L, tag), tag, tag});
  }
  SB();
  STAMP(8);
  LO(0);
  {
    f32x4 acc = {0, 0, 0, 0};
#pragma unroll
    for (int j = 0; j < 8; ++j) {
      pin(W1[j]);
      if (j >= 3) LO(j - 2);
      acc = mfma(AX[j], W1[j], acc);
      SB();
    }
    STAMP(9);
    lput<f32x4>(L_RH + vxs, acc);
    lbar();
    const f32x4 v = (lget<f32x4>(l_rh) + lget<f32x4>(l_rh + 1024)) + (lget<f32x4>(l_rh + 2048) + lget<f32x4>(l_rh + 3072));
    stc4(rw, a_hst, pk4(v, tag));
  }
  SB();
  STAMP(10);
  u32x4 H[2], H2[2];
  {
    pin(WO[0]); LO(6);
    pin(WO[1]); LO(7);
#pragma unroll
    for (int j = 0; j < POPOS; ++j) { pin(WO[2 + j]); L2(j); }
    u32x4 MA = ldc(rw, a_mlld, 0), PA0 = ldc(rw, a_pold, 0), PA1 = ldc(rw, a_pold, 8 * 4096);
    H[0] = ldc(rw, a_hld, 0);
    H[1] = ldc(rw, a_hld + 1024, 0);
    SB();
#pragma unroll
    for (int j = POPOS; j < 8; ++j) { if (j + 2 - POBURST < 8 && j + 2 - POBURST >= POPOS + 2) pin(WO[j + 2 - POBURST]); L2(j); }
    float y;
    auto ored = [&]() __attribute__((always_inline)) -> u32 {
      const u32x4 ml = xt(MA, tag);
      const float m = asf(ml[0]);
      float M = fmaxf(m, dpp<XOR1>(m));
      M = fmaxf(M, dpp<XOR2>(M));
      M = fmaxf(M, dpp<ROR8>(M));
      M = max32(max16(M));
      const float e = ex2((m - M) * LOG2E);
      float L = e * asf(ml[1]);
      L += dpp<XOR1>(L); L += dpp<XOR2>(L); L += dpp<ROR8>(L);
      L = tr16(L, L);
      L = tr32(L, L);
      const float e0 = dpp<0x00>(e), e1 = dpp<0x55>(e);
      const u32x4 t0 = xt(PA0, tag), t1 = xt(PA1, tag);
      const f32x4 o = e0 * asf4(t0) + e1 * asf4(t1);
      y = tr16(tr32(o[0], o[2]), tr32(o[1], o[3]));
      y += dpp<ROR8>(y);
      y *= __builtin_amdgcn_rcpf(L);
      asm volatile("" : "+v"(y));
      return or4(ml) | or4(t0) | or4(t1);
    };
    const u32 bad0 = ored();
    STAMP(11);
    if (UNLIKELY(any_bad(bad0))) {
      u32 spin = 0, bad;
      do {
        asm volatile("" ::: "memory");
        MA = ldc(rw, a_mlld, 0); PA0 = ldc(rw, a_pold, 0); PA1 = ldc(rw, a_pold, 8 * 4096);
        bad = ored();
      } while (any_bad(bad) && ++spin < SPIN_CAP);
    }
    lput<float>(l_pw, y);
    SB();
    H2[0] = ldc(rw, a_hld, 0);
    H2[1] = ldc(rw, a_hld + 1024, 0);
    SB();
    lbar();
    const f32x4 oa = lget<f32x4>(l_pr) + lget<f32x4>(l_pr + 128), ob = lget<f32x4>(l_pr + 16) + lget<f32x4>(l_pr + 144);
    f32x4 acc = {0, 0, 0, 0};
#pragma unroll
    for (int j = 0; j < 8; ++j) {
      const float t = j < 4 ? oa[j & 3] : ob[j & 3];
      acc = mfma(onehot(cvt2(t, t), mk4), WO[j], acc);
    }
    stc4(rw, a_sst, pk4(acc, tag));
  }
  SB();
  STAMP(12);
  if (has2) {
    u32 hd[8];
    auto hred = [&](const u32x4 (&X)[2]) __attribute__((always_inline)) -> u32 {
      const u32x4 t0 = xt(X[0], tag), t1 = xt(X[1], tag);
#pragma unroll
      for (int j = 0; j < 8; ++j) { const float t = fmaxf(sum8(asf(j < 4 ? t0[j & 3] : t1[j & 3])), 0.0f); hd[j] = cvt2(t, t); keep(hd[j]); }
      return or4(t0) | or4(t1);
    };
    if (UNLIKELY(any_bad(hred(H)))) {
      asm volatile("" ::: "memory");
      if (any_bad(hred(H2))) {
        u32 spin = 0, bad;
        do {
          asm volatile("" ::: "memory");
          H[0] = ldc(rw, a_hld, 0);
          H[1] = ldc(rw, a_hld + 1024, 0);
          bad = hred(H);
        } while (any_bad(bad) && ++spin < SPIN_CAP);
      }
    }
    STAMP(13);
    u32x4 AH[8];
#pragma unroll
    for (int j = 0; j < 8; ++j) { AH[j] = onehot(hd[j], mk4); keep4(AH[j]); }
    SB();
    f32x4 acc = {0, 0, 0, 0};
#pragma unroll
    for (int j = 0; j < 8; ++j) acc = mfma(AH[j], W2[j], acc);
    stc4(rw, a_gst, pk4(acc, tag));
  }
  SB();
  STAMP(14);
  if (w != 0) { pin(H2[0]); pin(H2[1]); }
  if (w == 0) {
    const u32 gt = lane < 32 ? tag : 0u;
    const u32 a_fg = lane < 32 ? a_fs : OOB;
    u32x4 S[4], G[4];
    f32x4 s, gs;
    auto ldS = [&]() __attribute__((always_inline)) {
#pragma unroll
      for (int i = 0; i < 4; ++i) S[i] = ldc(rw, a_fs, i * 32 * 8192);
    };
    auto ldG = [&]() __attribute__((always_inline)) {
#pragma unroll
      for (int i = 0; i < 4; ++i) G[i] = ldc(rw, i < 3 ? a_fs : a_fg, (4 + i) * 32 * 8192);
    };
    auto red = [&](const u32x4 (&X)[4], u32 lt, f32x4& r) __attribute__((always_inline)) -> u32 {
      const u32x4 t0 = xt(X[0], tag), t1 = xt(X[1], tag), t2 = xt(X[2], tag), t3 = xt(X[3], lt);
      r = (asf4(t0) + asf4(t1)) + (asf4(t2) + asf4(t3));
      asm volatile("" : "+v"(r));
      return or4(t0) | or4(t1) | or4(t2) | or4(t3);
    };
    ldS(); ldG();
    SB();
    u32 spin = 0;
    if (UNLIKELY(any_bad(red(S, tag, s)))) {
      u32 bad;
      do {
        asm volatile("" ::: "memory");
        ldS();
        bad = red(S, tag, s);
      } while (any_bad(bad) && ++spin < SPIN_CAP);
    }
    while (any_bad(red(G, gt, gs)) && ++spin < SPIN_CAP) {
      asm volatile("" ::: "memory");
      ldG();
    }
    pin(H2[0]); pin(H2[1]);
    float u[4];
#pragma unroll
    for (int r = 0; r < 4; ++r) u[r] = tr32(s[r], gs[r]);
    float y0 = tr16(u[0], u[2]), y1 = tr16(u[1], u[3]);
    y0 += dpp<XOR2>(y0); y1 += dpp<XOR2>(y1);
    y0 += dpp<ROR4>(y0); y1 += dpp<ROR4>(y1);
    y0 += dpp<ROR8>(y0); y1 += dpp<ROR8>(y1);
    const u32 yb = cvt2(y0, y1);
    auto sw = __builtin_amdgcn_permlane32_swap(yb, yb, false, false);
    const float g0 = blo(sw[1]), g1 = bhi(sw[1]);
    const u32 sg = cvt2(g0 * __builtin_amdgcn_rcpf(1.0f + ex2(-LOG2E * g0)), g1 * __builtin_amdgcn_rcpf(1.0f + ex2(-LOG2E * g1)));
    const u32 pr = cvt2(blo(sw[0]) * blo(sg), bhi(sw[0]) * bhi(sg));
    const u32 res = cvt2(blo(xf) + blo(pr), bhi(xf) + bhi(pr));
    if ((lane & 46) == 0) out[(n & 1) * 512 + p * 2 + (g & 1)] = res;
    if (p == 0 && lane == 0) __hip_atomic_store((u32*)ws, tag, __ATOMIC_RELAXED, __HIP_MEMORY_SCOPE_AGENT);
  }
  for (int i = 0; i < 100000 && __builtin_readsteadycounter() - stamp[0] < 3000; ++i) __builtin_amdgcn_s_sleep(1);
}

extern "C" void kernel_launch(void* const* d_in, const int* in_sizes, int n_in, void* d_out, int out_size, void* d_ws,
                              size_t ws_size, hipStream_t stream) {
  (void)in_sizes; (void)n_in; (void)out_size; (void)ws_size;
  (void)hipFuncSetAttribute((const void*)fused_kernel, hipFuncAttributeMaxDynamicSharedMemorySize, LDS_BYTES);
  fused_kernel<<<256, 256, LDS_BYTES, stream>>>(d_in[0], d_in[3], d_in[1], d_ws, d_in[2], d_in[6], d_in[4], d_in[7], (u32*)d_out);
}
```

```cpp
#include <hip/hip_runtime.h>
#include <hip/hip_bf16.h>
#include <stdint.h>

typedef unsigned int u32;
typedef unsigned long long u64;
typedef u32 u32x4 __attribute__((ext_vector_type(4)));
typedef u32 u32x2 __attribute__((ext_vector_type(2)));
typedef float f32x4 __attribute__((ext_vector_type(4)));
typedef float f32x2 __attribute__((ext_vector_type(2)));
typedef short bf16x8 __attribute__((ext_vector_type(8)));
typedef __bf16 bf16x2 __attribute__((ext_vector_type(2)));
typedef __amdgpu_buffer_rsrc_t rsrc_t;
#define DI __device__ __forceinline__
#define SB() __builtin_amdgcn_sched_barrier(0)
#define UNLIKELY(x) __builtin_expect(!!(x), 0)

constexpr u32 QP_OFF = 4096;
constexpr u32 HP_OFF = QP_OFF + 32 * 2 * 8 * 512;
constexpr u32 PO_OFF = HP_OFF + 112 * 2048;
constexpr u32 ML_OFF = PO_OFF + 256 * 4096;
constexpr u32 SP_OFF = ML_OFF + 32 * 1024;
constexpr u32 GP_OFF = SP_OFF + 128 * 8192;
constexpr u32 WS_USED = GP_OFF + 128 * 8192;
#define LB 0
#define JX 0
#define QX 0
#define KX 0
#define CD 8
#define EP 2
#define FS1 7
#define FD1 4
#define CPF 1
#define QPOL "sc1"
#define WORD 0
#define KORD 2
#define KIL 1
constexpr int KWS = KIL ? 262144 : 8192, KJS = KIL ? 8192 : 32768;
#define TH 0
#define FS 7
#define FD 0
#define RD 0
#define STA 16
#define E1 24
#define WIN 24
#define VA 32
#define PB 48
#define HB 56
#define HX 16
#define MLF 0
#define WT2 12
#define WIN2 WIN
#define WT 16
constexpr u32 OOB = 0x80000000u;
constexpr u32 SPIN_CAP = 1u << 16;
#define STAMP(i) do { } while (0)

constexpr int L_K = 0, L_V = 32768, L_1 = 65536, L_QG = 98304, L_WO = L_K, L_W2 = L_V;
constexpr int L_Z = 131072;
constexpr int L_S = L_Z + 16384;
constexpr int L_ST = L_S;
constexpr int L_F = L_S + 64;
constexpr int L_PG = L_S + 80;
constexpr int L_F2 = L_S + 96;
constexpr int L_TG = L_S + 112;
constexpr int L_XF = L_S + 128;
constexpr int L_PX = L_S + 256;
constexpr int L_ML = L_S + 768;
constexpr int L_RQ = L_S + 1024;
constexpr int L_Q = L_RQ + 4096;
constexpr int L_END = L_Q + 2304;
constexpr int LDS_BYTES = 160 * 1024;
static_assert(L_END + 1024 <= LDS_BYTES, "lds");
constexpr int L_GO = L_Z, L_GM = L_Z + 8192, L_GH = L_Z + 9216, L_GX = L_GH + 2048, L_DM = L_END + 256;
constexpr int G_Q = 32 + VA, P_Q = G_Q + 32, P_K = 32, P_V = 96, P_1 = 128;
static_assert(VA == 32, "V lands before the q gather");
constexpr int PBS[] = {PB}, HBS[] = {HB}, NRO = sizeof(PBS) / 4, NRH = sizeof(HBS) / 4, NLOAD = P_1 + 56 + 9 * NRO + 4 * NRH;
struct Seq { int ty[NLOAD + 1], ix[NLOAD + 1], po[NRO], ph[NRH], pwo, pw2; };
#define LPB 32
constexpr Seq mkseq(int late) {
  Seq q{};
  int n = 0;
  for (int s = 0; s <= 152; ++s) {
    if (s == G_Q) for (int i = 0; i < 32; ++i) { q.ty[n] = 1; q.ix[n++] = i; }
    if (s == 128) q.pwo = n;
    if (s == 152) q.pw2 = n;
    for (int r = 0; r < NRO; ++r) if (s == 96 + (late ? LPB : PBS[r])) { for (int i = 0; i < 9; ++i) { q.ty[n] = 2; q.ix[n++] = r * 16 + i; } q.po[r] = n; }
    for (int r = 0; r < NRH; ++r) if (s == 96 + HBS[r]) { for (int i = 0; i < 4; ++i) { q.ty[n] = 3; q.ix[n++] = r * 16 + i; } q.ph[r] = n; }
    if (s < 152) { q.ty[n] = 0; q.ix[n++] = s; }
  }
  return q;
}
constexpr Seq SQ = mkseq(0), SL = mkseq(1);
constexpr const Seq& sq(int M) { return M == 1 ? SL : SQ; }
static_assert(NRO == 1 && NRH == 1 && LPB >= 32 && SL.pwo == SQ.pwo && SL.pw2 == SQ.pw2 && SL.ph[0] == SQ.ph[0], "late order");
constexpr int P_WO = SQ.pwo, P_W2 = SQ.pw2, P_G2 = P_WO;
static_assert(SQ.po[0] - 9 > P_1 && SQ.ph[0] - 4 > P_1 && P_G2 - 62 > G_Q && P_1 - 62 > P_K, "gates");

DI rsrc_t mk(const void* p, u32 off, u32 bytes) { return __builtin_amdgcn_make_buffer_rsrc((char*)p + off, 0, bytes, 0x00020000); }
DI u32x4 ldw(rsrc_t r, u32 v, u32 s) { return __builtin_amdgcn_raw_buffer_load_b128(r, v, s, 2); }
DI u32x4 ldc(rsrc_t r, u32 v, u32 s) { return __builtin_amdgcn_raw_buffer_load_b128(r, v, s, 16); }
DI void stc4(rsrc_t r, u32 off, u32x4 v, u32 so = 0) { __builtin_amdgcn_raw_buffer_store_b128(v, r, off, so, STA); }
DI void stc2(rsrc_t r, u32 off, u32x2 v) { __builtin_amdgcn_raw_buffer_store_b64(v, r, off, 0, 16); }
DI void stc1(rsrc_t r, u32 off, u32 v) { __builtin_amdgcn_raw_buffer_store_b32(v, r, off, 0, 16); }
DI float asf(u32 u) { return __builtin_bit_cast(float, u); }
DI u32 asu(float f) { return __builtin_bit_cast(u32, f); }
DI u32 cvt2(float a, float b) { return __builtin_bit_cast(u32, __builtin_convertvector(f32x2{a, b}, bf16x2)); }
DI float blo(u32 u) { return asf(u << 16); }
DI float bhi(u32 u) { return asf(u & 0xFFFF0000u); }
DI float rbf(float f) { return blo(cvt2(f, f)); }
DI f32x4 mfma(u32x4 a, u32x4 b, f32x4 c) {
  return __builtin_amdgcn_mfma_f32_16x16x32_bf16(__builtin_bit_cast(bf16x8, a), __builtin_bit_cast(bf16x8, b), c, 0, 0, 0);
}
template <int CTRL> DI float dpp(float x) {
  return __builtin_bit_cast(float, __builtin_amdgcn_mov_dpp(__builtin_bit_cast(int, x), CTRL, 0xf, 0xf, true));
}
constexpr int XOR1 = 0xB1, XOR2 = 0x4E, ROR8 = 0x128, ROR4 = 0x124, HMIR = 0x141;
DI float tr32(float a, float b) { auto s = __builtin_amdgcn_permlane32_swap(asu(a), asu(b), false, false); return asf(s[0]) + asf(s[1]); }
DI float tr16(float a, float b) { auto s = __builtin_amdgcn_permlane16_swap(asu(a), asu(b), false, false); return asf(s[0]) + asf(s[1]); }
DI float max16(float x) { auto s = __builtin_amdgcn_permlane16_swap(asu(x), asu(x), false, false); return fmaxf(asf(s[0]), asf(s[1])); }
DI float max32(float x) { auto s = __builtin_amdgcn_permlane32_swap(asu(x), asu(x), false, false); return fmaxf(asf(s[0]), asf(s[1])); }
DI float sum8(float x) { x += dpp<XOR1>(x); x += dpp<XOR2>(x); x += dpp<HMIR>(x); return x; }
DI float max8(float x) { x = fmaxf(x, dpp<XOR1>(x)); x = fmaxf(x, dpp<XOR2>(x)); x = fmaxf(x, dpp<HMIR>(x)); return x; }
DI void lbar() { asm volatile("s_waitcnt lgkmcnt(0)" ::: "memory"); __builtin_amdgcn_s_barrier(); asm volatile("" ::: "memory"); }
DI void pin(u32x4 v) { asm volatile("" ::"v"(v)); }
DI void keep(u32& v) { asm volatile("" : "+v"(v)); }
DI void keep4(u32x4& v) { asm volatile("" : "+v"(v)); }
DI float ex2(float x) { return __builtin_amdgcn_exp2f(x); }
constexpr float LOG2E = 1.4426950408889634f;

DI u32 pk(float a, u32 tag) { return ((asu(a) + 0x800u) & 0xFFFFF000u) | tag; }
DI u32 df2(u32 a, u32 b, u32 tag) { return __builtin_amdgcn_bitop3_b32(a, b, tag, 0x7E); }
DI u32 df4(u32x4 v, u32 tag) { return df2(v[0], v[1], tag) | df2(v[2], v[3], tag); }
DI bool any_bad(u32 acc) { return __builtin_amdgcn_ballot_w64((acc & 0xFFFu) != 0) != 0; }
DI u32 or4(u32x4 v) { return (v[0] | v[1]) | (v[2] | v[3]); }
DI u32x4 onehot(u32 dup, const u32x4 mk4) { return u32x4{dup, dup, dup, dup} & mk4; }

extern __shared__ __attribute__((aligned(16))) char smem[];
template <class T> DI T lget(int off) { return *(const T*)(smem + off); }
template <class T> DI void lput(int off, T v) { *(T*)(smem + off) = v; }

DI u32x4 pk4(f32x4 v, u32 tag) { return u32x4{pk(v[0], tag), pk(v[1], tag), pk(v[2], tag), pk(v[3], tag)}; }
DI u32x4 xt(u32x4 v, u32 tag) { return v ^ u32x4{tag, tag, tag, tag}; }
DI f32x4 asf4(u32x4 v) { return __builtin_bit_cast(f32x4, v); }

DI u32x4 mks(const void* p, u32 off, u32 bytes) {
  const u64 a = (u64)p + off;
  return u32x4{(u32)a, (u32)(a >> 32) & 0xffffu, bytes, 0x00020000u};
}
template <int LA, int SO> DI void dma(u32 v, u32x4 srd) {
  u32 t;
  asm volatile("s_mov_b32 m0, %3\n\ts_mov_b32 %0, %4\n\tbuffer_load_dwordx4 %1, %2, %0 offen nt lds"
               : "=&s"(t) : "v"(v), "s"(srd), "n"(LA), "n"(SO) : "memory");
}
template <int LA> DI void dmas(u32 v, u32x4 srd, u32 so) {
  asm volatile("s_mov_b32 m0, %3\n\ts_nop 0\n\tbuffer_load_dwordx4 %0, %1, %2 offen nt lds" ::"v"(v), "s"(srd), "s"(so), "n"(LA) : "memory");
}
template <int LA, int SO> DI void dmac(u32 v, u32x4 srd) {
  u32 t;
  asm volatile("s_mov_b32 m0, %3\n\ts_mov_b32 %0, %4\n\tbuffer_load_dwordx4 %1, %2, %0 offen sc1 lds"
               : "=&s"(t) : "v"(v), "s"(srd), "n"(LA), "n"(SO) : "memory");
}
template <int LA, int SO> DI void dmap(u32 v, u32x4 srd) {
  u32 t;
  asm volatile("s_mov_b32 m0, %3\n\ts_mov_b32 %0, %4\n\tbuffer_load_dwordx4 %1, %2, %0 offen " QPOL " lds"
               : "=&s"(t) : "v"(v), "s"(srd), "n"(LA), "n"(SO) : "memory");
}
struct Ld { u32 t0, vk, vv, v1, vo, v2a, v2b, sa, sb, gq, go, go1, gm, gh0, gh1, gx; u32x4 rk, rv, r1, ro, r2, rw; };
constexpr bool isg(int I, int M) { return sq(M).ty[I] != 0; }
constexpr bool ismark(int D, int M) {
  if (D == P_K || D == G_Q || D == P_Q || D == P_V || D == P_1 || D == P_WO || D == P_W2) return true;
  for (int r = 0; r < NRO; ++r) if (D == sq(M).po[r]) return true;
  for (int r = 0; r < NRH; ++r) if (D == SQ.ph[r]) return true;
  return false;
}
constexpr u32 LSPIN = 1u << 22;
DI u32 minstep() {
  asm volatile("" ::: "memory");
  const u32x4 f = lget<u32x4>(L_F);
  return __builtin_amdgcn_readfirstlane(min(min(f[0], f[1]), min(f[2], f[3])));
}
template <int I, int M> DI void issue(Ld& l) {
  constexpr int ty = sq(M).ty[I], i = ty == 1 ? sq(M).ix[I] : sq(M).ix[I] & 15, r = sq(M).ix[I] >> 4;
  if constexpr (ty == 1) {
    dmap<L_QG + i * 1024, (i >> 3) * 16384 + (i & 7) * 512>(l.gq, l.rw);
  } else if constexpr (ty == 2) {
    if constexpr (i == 0 && NRO + NRH > 2) l.rw[2] = (r > 0 && minstep() >= 5) ? 0u : WS_USED;
    if constexpr (i == 8) dmac<L_GM, 0>(l.gm, l.rw);
    else dmac<L_GO + i * 1024, i * 8 * 4096>(i & 1 ? l.go1 : l.go, l.rw);
  } else if constexpr (ty == 3) {
    if constexpr (i == 0 && NRO + NRH > 2) l.rw[2] = (r > 0 && minstep() >= 6) ? 0u : WS_USED;
    if constexpr (i == 0) dmac<L_GH, 0>(l.gh0, l.rw);
    if constexpr (i == 1) dmac<L_GH + 1024, 0>(l.gh1, l.rw);
    if constexpr (i >= 2) dmac<L_GX + (i - 2) * 1024, (i - 2) * 16>(l.gx, l.rw);
  } else {
    constexpr int s = sq(M).ix[I], t = s / 32, w = s & 3, j = (s % 32) >> 2;
    constexpr int kw = KORD == 0 ? w : (KORD == 1 ? (s >> 1) & 3 : (s >> 3) & 3);
    constexpr int kj = KORD == 0 ? j : (KORD == 1 ? ((s & 1) << 2) | ((s >> 3) & 3) : ((s & 1) << 2) | (s & 2) | ((s >> 2) & 1));
    constexpr int vw = KORD < 2 ? w : (s >> 3) & 3, vj = KORD < 2 ? j : ((s & 3) << 1) | ((s >> 2) & 1);
    if constexpr (t == 0) dma<L_K + (kw * 8 + kj) * 1024, kw * KWS + ((kj >> 1) & 1) * 512 + (kj >> 2) * 128 + (kj & 1) * 4096>(l.vk, l.rk);
    if constexpr (t == 1) dma<L_V + (vw * 8 + vj) * 1024, vw * KWS + (vj >> 1) * 1024 + (vj & 1) * 4096>(l.vv, l.rv);
    constexpr int w1 = WORD ? (s >> 3) & 3 : w, j1 = WORD ? s & 7 : j;
    if constexpr (t == 2) dmas<L_1 + (w1 * 8 + j1) * 1024>(l.v1, l.r1, w1 * l.sa + j1 * l.sb);
    if constexpr (t == 3) dma<L_WO + (w * 8 + j) * 1024, w * 256 + (j & 1) * 2048 + (j >> 1) * 16384>(l.vo, l.ro);
    if constexpr (t == 4) dma<L_W2 + (w * 6 + j) * 1024, w * 256 + (j & 3) * 2048>(j < 4 ? l.v2a : l.v2b, l.r2);
  }
}
template <int A, int B, int M = 0> DI void issr(Ld& l) {
  if constexpr (A < B) {
    if constexpr (A == P_1 || A == P_G2) {
      u32 spin = 0, v;
      do {
        asm volatile("" ::: "memory");
        const u32x4 f = lget<u32x4>(L_F2);
        v = __builtin_amdgcn_readfirstlane((f[0] & f[1]) & (f[2] & f[3]));
      } while ((v & (A == P_1 ? 1 : 2)) == 0 && ++spin < LSPIN);
      asm volatile("" ::: "memory");
    }
    issue<A, M>(l);
    issr<A + 1, B, M>(l);
  }
}
#define LD 8
#define TL 645
#define LWIN 40
#define LW2 24
#define LW3 20
constexpr int upto(int D, int I, int M) {
  int n = I, fl = 0;
  for (int i = D; i < I; ++i) fl += isg(i, M) ? 0 : 1;
  while (n < NLOAD && n - D < 62) {
    if (n == G_Q && n - D > WT) break;
    if (sq(M).ty[n] >= 2 && (sq(M).ix[n] & 15) == 0 && n - D > (M == 1 ? (SL.ty[n] == 2 ? LW2 : LW3) : WT2)) break;
    if (!isg(n, M)) { if (fl >= (M == 1 ? LWIN : (D >= P_1 ? WIN2 : WIN))) break; ++fl; }
    ++n;
  }
  return n;
}
constexpr int nextd(int D, int M) {
  int n = D + 1, c = 1;
  while (n < NLOAD && !ismark(n, M) && (c < 4 || isg(n - 1, M))) { c += isg(n, M) ? 0 : 1; ++n; }
  return n;
}
template <int D, int I, int M = 0> DI void lrun(Ld& l, u32 lane, u64* stamp) {
  if constexpr (M == 0 && D >= P_1 + (LD)) {
    static_assert(I <= P_1 + LPB && I <= P_1 + PB, "common prefix");
    asm volatile("s_waitcnt vmcnt(%0)" ::"n"(I - D) : "memory");
    if ((u32)__builtin_readsteadycounter() - l.t0 > TL) lrun<D, I, 1>(l, lane, stamp);
    else lrun<D, I, 2>(l, lane, stamp);
  } else if constexpr (D <= NLOAD) {
    static_assert(I >= D && I - D < 64, "vmcnt");
    asm volatile("s_waitcnt vmcnt(%0)" ::"n"(I - D) : "memory");
    constexpr int N = upto(D, I, M);
    if constexpr (ismark(D, M)) {
      if (lane == 0) lput<u32>(L_PG, D == sq(M).po[0] ? SQ.po[0] : D);
      asm volatile("" ::: "memory");
    }
    if constexpr (D == P_K) STAMP(2);
    if constexpr (D == P_V) STAMP(3);
    if constexpr (D == P_1) STAMP(4);
    if constexpr (D == P_WO) STAMP(5);
    if constexpr (D == P_W2) STAMP(6);
    if constexpr (I <= G_Q && N > G_Q) STAMP(8);
    if constexpr (I < sq(M).po[0] && N >= sq(M).po[0]) STAMP(10);
    if constexpr (D == sq(M).po[0]) STAMP(11);
    if constexpr (D == 4) STAMP(12);
    if constexpr (D == SQ.ph[0]) STAMP(13);
    issr<I, N, M>(l);
    lrun<(D == NLOAD ? NLOAD + 1 : (nextd(D, M) < N ? nextd(D, M) : N)), N, M>(l, lane, stamp);
  }
}
template <int NEED> DI void waitp(u32 fa) {
  u32 t, cn;
  asm volatile("s_mov_b32 %1, %4\n1:\n\tds_read_b32 %0, %2 offset:16\n\ts_sub_u32 %1, %1, 1\n\ts_cbranch_scc1 2f\n\ts_waitcnt lgkmcnt(0)\n\t"
               "v_cmp_gt_u32 vcc, %3, %0\n\ts_cbranch_vccnz 1b\n2:\n\ts_waitcnt lgkmcnt(0)"
               : "=&v"(t), "=&s"(cn) : "v"(fa & ~15u), "n"(NEED), "n"(LSPIN) : "memory", "vcc", "scc");
}
#define SY_HEAD "s_mov_b32 %[cn], %[sp]\n\tds_write_b32 %[fl], %[st]\n1:\n\tds_read_b32 %[t], %[fa]\n\t"
#define SY_RD(i) "ds_read_b128 %[r" #i "], %[a" #i "] offset:%[o" #i "]\n\t"
#define SY_TAIL(n) "s_sub_u32 %[cn], %[cn], 1\n\ts_cbranch_scc1 2f\n\ts_waitcnt lgkmcnt(" #n ")\n\tv_cmp_gt_u32 vcc, %[st], %[t]\n\ts_cbranch_vccnz 1b\n2:\n\ts_waitcnt lgkmcnt(0)"
template <int O0, int O1, int O2, int O3> DI void sync4(u32 st, u32 fl, u32 fa, u32 a0, u32 a1, u32x4 (&r)[4]) {
  u32 t, cn;
  asm volatile(SY_HEAD SY_RD(0) SY_RD(1) SY_RD(2) SY_RD(3) SY_TAIL(4)
               : [t] "=&v"(t), [cn] "=&s"(cn), [r0] "=&v"(r[0]), [r1] "=&v"(r[1]), [r2] "=&v"(r[2]), [r3] "=&v"(r[3])
               : [st] "v"(st), [fl] "v"(fl), [fa] "v"(fa), [a0] "v"(a0), [a1] "v"(a1), [a2] "v"(a0), [a3] "v"(a1), [o0] "n"(O0), [o1] "n"(O1),
                 [o2] "n"(O2), [o3] "n"(O3), [sp] "n"(LSPIN)
               : "memory", "vcc", "scc");
}
DI void sync6(u32 st, u32 fl, u32 fa, u32 am, u32 ao, u32x4 (&r)[6]) {
  u32 t, cn;
  asm volatile(SY_HEAD SY_RD(0) SY_RD(1) SY_RD(2) SY_RD(3) SY_RD(4) SY_RD(5) SY_TAIL(6)
               : [t] "=&v"(t), [cn] "=&s"(cn), [r0] "=&v"(r[0]), [r1] "=&v"(r[1]), [r2] "=&v"(r[2]), [r3] "=&v"(r[3]), [r4] "=&v"(r[4]), [r5] "=&v"(r[5])
               : [st] "v"(st), [fl] "v"(fl), [fa] "v"(fa), [a0] "v"(am), [a1] "v"(am), [a2] "v"(ao), [a3] "v"(ao), [a4] "v"(ao), [a5] "v"(ao),
                 [o0] "n"(0), [o1] "n"(16), [o2] "n"(0), [o3] "n"(8192), [o4] "n"(16384), [o5] "n"(24576), [sp] "n"(LSPIN)
               : "memory", "vcc", "scc");
}
DI float max4(float a, float b, float c, float d) {
  float m;
  asm("v_max3_f32 %0, %1, %2, %3\n\tv_max_f32 %0, %0, %4" : "=&v"(m) : "v"(a), "v"(b), "v"(c), "v"(d));
  return m;
}
DI float maxh(float m) {
  float t;
  asm("s_nop 1\n\tv_max_f32_dpp %0, %0, %0 row_ror:8 row_mask:0xf bank_mask:0xf\n\tv_mov_b32 %1, %0\n\ts_nop 1\n\tv_permlane16_swap_b32 %0, %1\n\t"
      "v_max_f32 %0, %0, %1\n\tv_mov_b32 %1, %0\n\ts_nop 1\n\tv_permlane32_swap_b32 %0, %1\n\tv_max_f32 %0, %0, %1"
      : "+v"(m), "=&v"(t));
  return m;
}
DI void again(u32x4& X, rsrc_t r, u32 v, u32 so, u32 tag) {
  if ((df4(X, tag) & 0xFFFu) != 0) X = ldc(r, v, so);
}

#define ST_s1
#define ST_s7
#define ST_s11
#define ST_s12
#define ST_s13
#define ST_wait
static_assert(L_WO == 0 && L_1 == 0x10000 && L_GX == L_GH + 0x800 && GP_OFF - SP_OFF == 0x100000 && L_F == L_S + 64 && L_PG == L_S + 80, "asm");
static_assert(NRO == 1 && NRH == 1, "asm");
#define TAIL \
  "s_nop 1\n\t" \
  "v_mov_b32 v2, %[ls]\n\t" \
  "v_mov_b32 v122, 0x800\n\t" \
  "v_mov_b32 v123, 0xfffff000\n\t" \
  "v_mov_b32 v124, 5\n\t" \
  "v_mov_b32 v125, 7\n\t" \
  "v_add_u32 v126, 0x10000, %[lt]\n\t" \
  "v_add_u32 v127, 0x800, %[h0]\n\t" \
  "s_mov_b32 %[t1], 0x100000\n\t" \
  "s_mov_b64 exec, 0\n\t" \
  "s_mov_b64 vcc, 0\n\t" \
  "s_branch .Ltb1\n\t" \
  ".Lta0:\n\t" \
  "s_mov_b64 exec, -1\n\t" \
  "s_mov_b32 %[cn], 0x400000\n\t" \
  ".Lta1:\n\t" \
  "ds_read_b32 v3, v2 offset:80\n\t" \
  "s_waitcnt lgkmcnt(0)\n\t" \
  "v_cmp_gt_u32 vcc, %[pwo], v3\n\t" \
  "s_cbranch_vccz .Lta2\n\t" \
  "s_sleep 1\n\t" \
  "s_sub_u32 %[cn], %[cn], 1\n\t" \
  "s_cbranch_scc0 .Lta1\n\t" \
  "s_mov_b32 %[cn], 0\n\t" \
  ".Lta2:\n\t" \
  "ds_read_b128 v[4:7], %[lt] offset:0\n\t" \
  "ds_read_b128 v[8:11], %[lt] offset:1024\n\t" \
  "ds_read_b128 v[12:15], %[lt] offset:2048\n\t" \
  "ds_read_b128 v[16:19], %[lt] offset:3072\n\t" \
  "ds_read_b128 v[20:23], %[lt] offset:4096\n\t" \
  "ds_read_b128 v[24:27], %[lt] offset:5120\n\t" \
  "ds_read_b128 v[28:31], %[lt] offset:6144\n\t" \
  "ds_read_b128 v[32:35], %[lt] offset:7168\n\t" \
  "s_cmp_lt_u32 %[jj], 28\n\t" \
  "s_cbranch_scc1 .Lta3\n\t" \
  "s_waitcnt lgkmcnt(4)\n\t" \
  "ds_read_b128 v[36:39], v126 offset:0\n\t" \
  "ds_read_b128 v[40:43], v126 offset:1024\n\t" \
  "ds_read_b128 v[44:47], v126 offset:2048\n\t" \
  "ds_read_b128 v[48:51], v126 offset:3072\n\t" \
  "ds_read_b128 v[52:55], v126 offset:4096\n\t" \
  "ds_read_b128 v[56:59], v126 offset:5120\n\t" \
  "ds_read_b128 v[60:63], v126 offset:6144\n\t" \
  "ds_read_b128 v[64:67], v126 offset:7168\n\t" \
  ".Lta3:\n\t" \
  "s_mov_b32 %[cn], 0x400000\n\t" \
  ".Ltb1:\n\t" \
  "ds_read_b32 v3, v2 offset:80\n\t" \
  "ds_read_b128 v[68:71], %[gm]\n\t" \
  "ds_read_b128 v[72:75], %[gm] offset:256\n\t" \
  "s_waitcnt lgkmcnt(0)\n\t" \
  "v_xor_b32 v130, v68, %[tg]\n\t" \
  "v_xor_b32 v131, v72, %[tg]\n\t" \
  "v_bitop3_b32 v138, v69, v73, %[tg] bitop3:0x7e\n\t" \
  "v_or3_b32 v138, v138, v130, v131\n\t" \
  "v_mul_f32 v130, 0x3fb8aa3b, v130\n\t" \
  "v_lshlrev_b32 v138, 20, v138\n\t" \
  "v_mul_f32 v131, 0x3fb8aa3b, v131\n\t" \
  "v_cmp_ne_u32 vcc, 0, v138\n\t" \
  "v_max_f32 v132, v130, v131\n\t" \
  "s_cbranch_vccnz .Ltb1s\n\t" \
  "ds_read_b128 v[76:79], %[go]\n\t" \
  "ds_read_b128 v[80:83], %[go] offset:2048\n\t" \
  ".Ltb3:\n\t" \
  ST_s11 \
  "v_max_f32_dpp v132, v132, v132 quad_perm:[1,0,3,2] row_mask:0xf bank_mask:0xf\n\t" \
  "v_bitop3_b32 v142, v70, v71, %[tg] bitop3:0x7e\n\t" \
  "v_bitop3_b32 v143, v74, v75, %[tg] bitop3:0x7e\n\t" \
  "v_max_f32_dpp v132, v132, v132 quad_perm:[2,3,0,1] row_mask:0xf bank_mask:0xf\n\t" \
  "v_or_b32 v142, v142, v143\n\t" \
  "v_xor_b32 v116, v69, %[tg]\n\t" \
  "v_max_f32_dpp v132, v132, v132 row_half_mirror row_mask:0xf bank_mask:0xf\n\t" \
  "v_xor_b32 v117, v73, %[tg]\n\t" \
  "s_nop 0\n\t" \
  "v_max_f32_dpp v132, v132, v132 row_mirror row_mask:0xf bank_mask:0xf\n\t" \
  "v_sub_f32 v128, v130, v132\n\t" \
  "v_sub_f32 v129, v131, v132\n\t" \
  "v_exp_f32 v128, v128\n\t" \
  "v_exp_f32 v129, v129\n\t" \
  "s_waitcnt lgkmcnt(1)\n\t" \
  "v_xor_b32 v108, v76, %[tg]\n\t" \
  "v_xor_b32 v109, v77, %[tg]\n\t" \
  "v_xor_b32 v110, v78, %[tg]\n\t" \
  "v_xor_b32 v111, v79, %[tg]\n\t" \
  "v_mul_f32 v133, v128, v116\n\t" \
  "v_pk_mul_f32 v[134:135], v[108:109], v[128:129] op_sel_hi:[1,0]\n\t" \
  "v_fmac_f32 v133, v129, v117\n\t" \
  "v_pk_mul_f32 v[136:137], v[110:111], v[128:129] op_sel_hi:[1,0]\n\t" \
  "s_waitcnt lgkmcnt(0)\n\t" \
  "v_xor_b32 v112, v80, %[tg]\n\t" \
  "v_xor_b32 v113, v81, %[tg]\n\t" \
  "v_xor_b32 v114, v82, %[tg]\n\t" \
  "v_xor_b32 v115, v83, %[tg]\n\t" \
  "v_add_f32_dpp v133, v133, v133 quad_perm:[1,0,3,2] row_mask:0xf bank_mask:0xf\n\t" \
  "v_pk_fma_f32 v[134:135], v[112:113], v[128:129], v[134:135] op_sel:[0,1,0]\n\t" \
  "v_pk_fma_f32 v[136:137], v[114:115], v[128:129], v[136:137] op_sel:[0,1,0]\n\t" \
  "v_add_f32_dpp v133, v133, v133 quad_perm:[2,3,0,1] row_mask:0xf bank_mask:0xf\n\t" \
  "v_add_f32_dpp v134, v134, v134 row_ror:8 row_mask:0xf bank_mask:0x3\n\t" \
  "v_add_f32_dpp v135, v135, v135 row_ror:8 row_mask:0xf bank_mask:0x3\n\t" \
  "v_add_f32_dpp v134, v136, v136 row_ror:8 row_mask:0xf bank_mask:0xc\n\t" \
  "v_add_f32_dpp v135, v137, v137 row_ror:8 row_mask:0xf bank_mask:0xc\n\t" \
  "v_add_f32_dpp v133, v133, v133 row_half_mirror row_mask:0xf bank_mask:0xf\n\t" \
  "v_add_f32_dpp v134, v134, v134 row_half_mirror row_mask:0xf bank_mask:0x5\n\t" \
  "v_add_f32_dpp v134, v135, v135 row_half_mirror row_mask:0xf bank_mask:0xa\n\t" \
  "v_add_f32_dpp v133, v133, v133 row_mirror row_mask:0xf bank_mask:0xf\n\t" \
  "v_or3_b32 v138, v108, v109, v110\n\t" \
  "v_add_f32_dpp v134, v134, v134 quad_perm:[1,0,3,2] row_mask:0xf bank_mask:0xf\n\t" \
  "v_rcp_f32 v133, v133\n\t" \
  "v_or3_b32 v139, v111, v112, v113\n\t" \
  "v_add_f32_dpp v134, v134, v134 quad_perm:[2,3,0,1] row_mask:0xf bank_mask:0xf\n\t" \
  "v_or3_b32 v142, v142, v114, v115\n\t" \
  "v_mul_f32 v134, v134, v133\n\t" \
  "v_or_b32 v138, v138, v139\n\t" \
  "v_cvt_pk_bf16_f32 v134, v134, v134\n\t" \
  "v_bitop3_b32 v138, v138, v142, v123 bitop3:0x54\n\t" \
  "ds_write_b32 %[pw], v134\n\t" \
  "v_cmp_ne_u32 vcc, 0, v138\n\t" \
  "s_cbranch_vccnz .Ltfl\n\t" \
  ".Ltb4:\n\t" \
  "ds_write_b32 %[fl], v124\n\t" \
  "s_mov_b32 %[cn], 0x400000\n\t" \
  ".Lte1:\n\t" \
  "ds_read_b128 v[68:71], v2 offset:64\n\t" \
  "ds_read_b128 v[84:87], %[pr]\n\t" \
  "ds_read_b128 v[88:91], %[pr] offset:16\n\t" \
  "s_waitcnt lgkmcnt(1)\n\t" \
  "v_min3_u32 v138, v68, v69, v70\n\t" \
  "v_and_b32 v92, v84, %[k0]\n\t" \
  "v_min_u32 v138, v138, v71\n\t" \
  "v_and_b32 v93, v84, %[k1]\n\t" \
  "v_cmp_gt_u32 vcc, 5, v138\n\t" \
  "v_and_b32 v94, v84, %[k2]\n\t" \
  "v_and_b32 v95, v84, %[k3]\n\t" \
  "s_cbranch_vccnz .Lte1s\n\t" \
  ".Lte2:\n\t" \
  "v_and_b32 v96, v85, %[k0]\n\t" \
  "v_and_b32 v97, v85, %[k1]\n\t" \
  "v_and_b32 v98, v85, %[k2]\n\t" \
  "v_and_b32 v99, v85, %[k3]\n\t" \
  "v_mfma_f32_16x16x32_bf16 v[100:103], v[92:95], v[4:7], 0\n\t" \
  "v_and_b32 v92, v86, %[k0]\n\t" \
  "v_and_b32 v93, v86, %[k1]\n\t" \
  "v_and_b32 v94, v86, %[k2]\n\t" \
  "v_and_b32 v95, v86, %[k3]\n\t" \
  "v_mfma_f32_16x16x32_bf16 v[100:103], v[96:99], v[8:11], v[100:103]\n\t" \
  "v_and_b32 v96, v87, %[k0]\n\t" \
  "v_and_b32 v97, v87, %[k1]\n\t" \
  "v_and_b32 v98, v87, %[k2]\n\t" \
  "v_and_b32 v99, v87, %[k3]\n\t" \
  "v_mfma_f32_16x16x32_bf16 v[100:103], v[92:95], v[12:15], v[100:103]\n\t" \
  "s_waitcnt lgkmcnt(0)\n\t" \
  "v_and_b32 v92, v88, %[k0]\n\t" \
  "v_and_b32 v93, v88, %[k1]\n\t" \
  "v_and_b32 v94, v88, %[k2]\n\t" \
  "v_and_b32 v95, v88, %[k3]\n\t" \
  "v_mfma_f32_16x16x32_bf16 v[100:103], v[96:99], v[16:19], v[100:103]\n\t" \
  "v_and_b32 v96, v89, %[k0]\n\t" \
  "v_and_b32 v97, v89, %[k1]\n\t" \
  "v_and_b32 v98, v89, %[k2]\n\t" \
  "v_and_b32 v99, v89, %[k3]\n\t" \
  "v_mfma_f32_16x16x32_bf16 v[100:103], v[92:95], v[20:23], v[100:103]\n\t" \
  "v_and_b32 v92, v90, %[k0]\n\t" \
  "v_and_b32 v93, v90, %[k1]\n\t" \
  "v_and_b32 v94, v90, %[k2]\n\t" \
  "v_and_b32 v95, v90, %[k3]\n\t" \
  "v_mfma_f32_16x16x32_bf16 v[100:103], v[96:99], v[24:27], v[100:103]\n\t" \
  "v_and_b32 v96, v91, %[k0]\n\t" \
  "v_and_b32 v97, v91, %[k1]\n\t" \
  "v_and_b32 v98, v91, %[k2]\n\t" \
  "v_and_b32 v99, v91, %[k3]\n\t" \
  "v_mfma_f32_16x16x32_bf16 v[100:103], v[92:95], v[28:31], v[100:103]\n\t" \
  "s_cmp_ge_u32 %[jj], 28\n\t" \
  "v_mfma_f32_16x16x32_bf16 v[100:103], v[96:99], v[32:35], v[100:103]\n\t" \
  "s_cbranch_scc1 .Ltx\n\t" \
  "ds_read_b32 v3, v2 offset:80\n\t" \
  "ds_read2_b32 v[108:109], %[h0] offset1:4\n\t" \
  "ds_read2_b32 v[110:111], %[h0] offset0:8 offset1:12\n\t" \
  "ds_read2_b32 v[120:121], %[h1] offset1:4\n\t" \
  "s_mov_b32 %[cn], 0x400000\n\t" \
  "s_nop 1\n\t" \
  "v_add_u32 v104, v100, v122\n\t" \
  "v_add_u32 v105, v101, v122\n\t" \
  "v_add_u32 v106, v102, v122\n\t" \
  "v_add_u32 v107, v103, v122\n\t" \
  "v_and_or_b32 v104, v104, v123, %[tg]\n\t" \
  "v_and_or_b32 v105, v105, v123, %[tg]\n\t" \
  "v_and_or_b32 v106, v106, v123, %[tg]\n\t" \
  "v_and_or_b32 v107, v107, v123, %[tg]\n\t" \
  "buffer_store_dwordx4 v[104:107], %[ast], %[rs], 0 offen sc1\n\t" \
  ST_s12 \
  ".Ltwm:\n\t" \
  "s_waitcnt lgkmcnt(0)\n\t" \
  "v_cmp_gt_u32 vcc, %[pw2], v3\n\t" \
  "s_cbranch_vccnz .Ltwms\n\t" \
  ".Ltwmg:\n\t" \
  "ds_read_b128 v[68:71], %[w2] offset:0\n\t" \
  "ds_read_b128 v[72:75], %[w2] offset:1024\n\t" \
  "ds_read_b128 v[76:79], %[w2] offset:2048\n\t" \
  "ds_read_b128 v[80:83], %[w2] offset:3072\n\t" \
  "ds_read_b128 v[84:87], %[w2] offset:4096\n\t" \
  "ds_read_b128 v[88:91], %[w2] offset:5120\n\t" \
  ".Ltcm:\n\t" \
  "v_xor_b32 v146, v108, %[tg]\n\t" \
  "v_xor_b32 v147, v109, %[tg]\n\t" \
  "v_xor_b32 v148, v110, %[tg]\n\t" \
  "v_xor_b32 v149, v111, %[tg]\n\t" \
  "v_xor_b32 v158, v120, %[tg]\n\t" \
  "v_xor_b32 v159, v121, %[tg]\n\t" \
  "v_pk_add_f32 v[132:133], v[146:147], v[148:149]\n\t" \
  "v_add_f32 v129, v158, v159\n\t" \
  "v_add_f32 v128, v132, v133\n\t" \
  "v_or3_b32 v138, v146, v147, v148\n\t" \
  "v_add_f32_dpp v129, v129, v129 quad_perm:[1,0,3,2] row_mask:0xf bank_mask:0xf\n\t" \
  "v_add_f32_dpp v128, v128, v128 quad_perm:[1,0,3,2] row_mask:0xf bank_mask:0xf\n\t" \
  "v_or3_b32 v139, v149, v158, v159\n\t" \
  "v_add_f32_dpp v129, v129, v129 quad_perm:[2,3,0,1] row_mask:0xf bank_mask:0xf\n\t" \
  "v_max_f32 v128, 0, v128\n\t" \
  "v_bitop3_b32 v138, v138, v139, v123 bitop3:0x54\n\t" \
  "v_cvt_pk_bf16_f32 v128, v128, v128\n\t" \
  "v_cmp_ne_u32 vcc, 0, v138\n\t" \
  "v_max_f32 v129, 0, v129\n\t" \
  "v_and_b32 v92, v128, %[k0]\n\t" \
  "v_and_b32_dpp v93, v128, %[k1] quad_perm:[2,3,0,1] row_mask:0xf bank_mask:0xf bound_ctrl:1\n\t" \
  "v_and_b32_dpp v94, v128, %[k2] row_shr:4 row_mask:0xf bank_mask:0xf bound_ctrl:1\n\t" \
  "v_and_b32_dpp v95, v128, %[k3] row_half_mirror row_mask:0xf bank_mask:0xf bound_ctrl:1\n\t" \
  "v_cvt_pk_bf16_f32 v129, v129, v129\n\t" \
  "s_cbranch_vccnz .Ltcms\n\t" \
  ".Ltcmg:\n\t" \
  ST_s13 \
  "s_waitcnt lgkmcnt(5)\n\t" \
  "v_mfma_f32_16x16x32_bf16 v[100:103], v[92:95], v[68:71], 0\n\t" \
  "v_and_b32 v97, v128, %[k1]\n\t" \
  "v_and_b32_dpp v96, v128, %[k0] quad_perm:[2,3,0,1] row_mask:0xf bank_mask:0xf bound_ctrl:1\n\t" \
  "v_and_b32_dpp v99, v128, %[k3] row_shr:4 row_mask:0xf bank_mask:0xf bound_ctrl:1\n\t" \
  "v_and_b32_dpp v98, v128, %[k2] row_half_mirror row_mask:0xf bank_mask:0xf bound_ctrl:1\n\t" \
  "s_waitcnt lgkmcnt(4)\n\t" \
  "s_nop 0\n\t" \
  "v_mfma_f32_16x16x32_bf16 v[100:103], v[96:99], v[72:75], v[100:103]\n\t" \
  "v_and_b32 v94, v128, %[k2]\n\t" \
  "v_and_b32_dpp v95, v128, %[k3] quad_perm:[2,3,0,1] row_mask:0xf bank_mask:0xf bound_ctrl:1\n\t" \
  "v_and_b32_dpp v92, v128, %[k0] row_shl:4 row_mask:0xf bank_mask:0xf bound_ctrl:1\n\t" \
  "v_and_b32_dpp v93, v128, %[k1] row_half_mirror row_mask:0xf bank_mask:0xf bound_ctrl:1\n\t" \
  "s_waitcnt lgkmcnt(3)\n\t" \
  "s_nop 0\n\t" \
  "v_mfma_f32_16x16x32_bf16 v[100:103], v[92:95], v[76:79], v[100:103]\n\t" \
  "v_and_b32 v99, v128, %[k3]\n\t" \
  "v_and_b32_dpp v98, v128, %[k2] quad_perm:[2,3,0,1] row_mask:0xf bank_mask:0xf bound_ctrl:1\n\t" \
  "v_and_b32_dpp v97, v128, %[k1] row_shl:4 row_mask:0xf bank_mask:0xf bound_ctrl:1\n\t" \
  "v_and_b32_dpp v96, v128, %[k0] row_half_mirror row_mask:0xf bank_mask:0xf bound_ctrl:1\n\t" \
  "s_waitcnt lgkmcnt(2)\n\t" \
  "s_nop 0\n\t" \
  "v_mfma_f32_16x16x32_bf16 v[100:103], v[96:99], v[80:83], v[100:103]\n\t" \
  "v_and_b32 v92, v129, %[k0]\n\t" \
  "v_and_b32 v93, v129, %[k1]\n\t" \
  "v_and_b32_dpp v94, v129, %[k2] row_shr:4 row_mask:0xf bank_mask:0xf bound_ctrl:1\n\t" \
  "v_and_b32_dpp v95, v129, %[k3] row_shr:4 row_mask:0xf bank_mask:0xf bound_ctrl:1\n\t" \
  "s_waitcnt lgkmcnt(1)\n\t" \
  "s_nop 0\n\t" \
  "v_mfma_f32_16x16x32_bf16 v[100:103], v[92:95], v[84:87], v[100:103]\n\t" \
  "v_and_b32 v98, v129, %[k2]\n\t" \
  "v_and_b32 v99, v129, %[k3]\n\t" \
  "v_and_b32_dpp v96, v129, %[k0] row_shl:4 row_mask:0xf bank_mask:0xf bound_ctrl:1\n\t" \
  "v_and_b32_dpp v97, v129, %[k1] row_shl:4 row_mask:0xf bank_mask:0xf bound_ctrl:1\n\t" \
  "s_waitcnt lgkmcnt(0)\n\t" \
  "s_nop 0\n\t" \
  "v_mfma_f32_16x16x32_bf16 v[100:103], v[96:99], v[88:91], v[100:103]\n\t" \
  "s_nop 7\n\t" \
  "v_add_u32 v104, v100, v122\n\t" \
  "v_add_u32 v105, v101, v122\n\t" \
  "v_add_u32 v106, v102, v122\n\t" \
  "v_add_u32 v107, v103, v122\n\t" \
  "v_and_or_b32 v104, v104, v123, %[tg]\n\t" \
  "v_and_or_b32 v105, v105, v123, %[tg]\n\t" \
  "v_and_or_b32 v106, v106, v123, %[tg]\n\t" \
  "v_and_or_b32 v107, v107, v123, %[tg]\n\t" \
  "buffer_store_dwordx4 v[104:107], %[ast], %[rs], %[t1] offen sc1\n\t" \
  "ds_write_b32 %[fl], v125\n\t" \
  "s_cbranch_execz .Lta0\n\t" \
  "s_branch .Ltend\n\t" \
  ".Ltwms:\n\t" \
  "ds_read_b32 v3, v2 offset:80\n\t" \
  "ds_read2_b32 v[108:109], %[h0] offset1:4\n\t" \
  "ds_read2_b32 v[110:111], %[h0] offset0:8 offset1:12\n\t" \
  "ds_read2_b32 v[120:121], %[h1] offset1:4\n\t" \
  "s_sub_u32 %[cn], %[cn], 1\n\t" \
  "s_cbranch_scc0 .Ltwm\n\t" \
  "s_mov_b32 %[cn], 0\n\t" \
  "s_branch .Ltwmg\n\t" \
  ".Ltcms:\n\t" \
  "v_cmp_gt_u32 vcc, %[ph], v3\n\t" \
  "s_cbranch_vccz .Lthm\n\t" \
  "ds_read_b32 v3, v2 offset:80\n\t" \
  "ds_read2_b32 v[108:109], %[h0] offset1:4\n\t" \
  "ds_read2_b32 v[110:111], %[h0] offset0:8 offset1:12\n\t" \
  "ds_read2_b32 v[120:121], %[h1] offset1:4\n\t" \
  "s_waitcnt lgkmcnt(0)\n\t" \
  "s_sub_u32 %[cn], %[cn], 1\n\t" \
  "s_cbranch_scc0 .Ltcm\n\t" \
  "s_mov_b32 %[cn], 0\n\t" \
  "s_branch .Ltcmg\n\t" \
  ".Lthm:\n\t" \
  "buffer_load_dword v108, %[ah0], %[rs], 0 offen offset:0 sc1\n\t" \
  "buffer_load_dword v109, %[ah0], %[rs], 0 offen offset:128 sc1\n\t" \
  "buffer_load_dword v110, %[ah0], %[rs], 0 offen offset:256 sc1\n\t" \
  "buffer_load_dword v111, %[ah0], %[rs], 0 offen offset:384 sc1\n\t" \
  "buffer_load_dword v120, %[ah1], %[rs], 0 offen offset:0 sc1\n\t" \
  "buffer_load_dword v121, %[ah1], %[rs], 0 offen offset:128 sc1\n\t" \
  "s_waitcnt vmcnt(0)\n\t" \
  "s_sub_u32 %[cn], %[cn], 64\n\t" \
  "s_cbranch_scc0 .Ltcm\n\t" \
  "s_mov_b32 %[cn], 0\n\t" \
  "s_branch .Ltcmg\n\t" \
  ".Ltb1s:\n\t" \
  "v_cmp_gt_u32 vcc, %[po], v3\n\t" \
  "s_cbranch_vccz .Ltfa\n\t" \
  "s_sub_u32 %[cn], %[cn], 1\n\t" \
  "s_cbranch_scc0 .Ltb1\n\t" \
  "s_mov_b32 %[cn], 0\n\t" \
  ".Ltfa:\n\t" \
  "ds_read_b128 v[76:79], %[go]\n\t" \
  "ds_read_b128 v[80:83], %[go] offset:2048\n\t" \
  "s_waitcnt lgkmcnt(0)\n\t" \
  ".Ltfl:\n\t" \
  "v_bitop3_b32 v138, v68, v69, %[tg] bitop3:0x7e\n\t" \
  "v_bitop3_b32 v144, v70, v71, %[tg] bitop3:0x7e\n\t" \
  "v_bitop3_b32 v139, v72, v73, %[tg] bitop3:0x7e\n\t" \
  "v_bitop3_b32 v145, v74, v75, %[tg] bitop3:0x7e\n\t" \
  "v_bitop3_b32 v140, v76, v77, %[tg] bitop3:0x7e\n\t" \
  "v_bitop3_b32 v141, v78, v79, %[tg] bitop3:0x7e\n\t" \
  "v_bitop3_b32 v142, v80, v81, %[tg] bitop3:0x7e\n\t" \
  "v_bitop3_b32 v143, v82, v83, %[tg] bitop3:0x7e\n\t" \
  "v_bitop3_b32 v138, v138, v144, v123 bitop3:0x54\n\t" \
  "v_bitop3_b32 v139, v139, v145, v123 bitop3:0x54\n\t" \
  "v_bitop3_b32 v140, v140, v141, v123 bitop3:0x54\n\t" \
  "v_bitop3_b32 v142, v142, v143, v123 bitop3:0x54\n\t" \
  "v_xor_b32 v130, v68, %[tg]\n\t" \
  "v_xor_b32 v131, v72, %[tg]\n\t" \
  "v_or3_b32 v141, v138, v139, v140\n\t" \
  "v_mul_f32 v130, 0x3fb8aa3b, v130\n\t" \
  "v_mul_f32 v131, 0x3fb8aa3b, v131\n\t" \
  "v_or_b32 v141, v141, v142\n\t" \
  "v_max_f32 v132, v130, v131\n\t" \
  "v_cmp_ne_u32 vcc, 0, v141\n\t" \
  "s_mov_b32 %[t0], 0x10000\n\t" \
  "s_cbranch_vccz .Ltb3\n\t" \
  "v_cmp_ne_u32 vcc, 0, v138\n\t" \
  "s_mov_b64 exec, vcc\n\t" \
  "buffer_load_dwordx4 v[68:71], %[aml], %[rs], 0 offen sc1\n\t" \
  "s_mov_b64 exec, -1\n\t" \
  "v_cmp_ne_u32 vcc, 0, v139\n\t" \
  "s_mov_b64 exec, vcc\n\t" \
  "buffer_load_dwordx4 v[72:75], %[aml], %[rs], 0 offen offset:%[mo] sc1\n\t" \
  "s_mov_b64 exec, -1\n\t" \
  "v_cmp_ne_u32 vcc, 0, v140\n\t" \
  "s_mov_b64 exec, vcc\n\t" \
  "buffer_load_dwordx4 v[76:79], %[apo], %[rs], 0 offen sc1\n\t" \
  "s_mov_b64 exec, -1\n\t" \
  "v_cmp_ne_u32 vcc, 0, v142\n\t" \
  "s_mov_b64 exec, vcc\n\t" \
  "buffer_load_dwordx4 v[80:83], %[apo], %[rs], %[t0] offen sc1\n\t" \
  "s_mov_b64 exec, -1\n\t" \
  "s_waitcnt vmcnt(0)\n\t" \
  "s_sub_u32 %[cn], %[cn], 64\n\t" \
  "s_cbranch_scc0 .Ltfl\n\t" \
  "s_mov_b32 %[cn], 0\n\t" \
  "s_branch .Ltb4\n\t" \
  ".Lte1s:\n\t" \
  "s_sub_u32 %[cn], %[cn], 1\n\t" \
  "s_cbranch_scc0 .Lte1\n\t" \
  "s_mov_b32 %[cn], 0\n\t" \
  "s_branch .Lte2\n\t" \
  ".Ltx:\n\t" \
  "v_add_u32 v126, 0x400, v127\n\t" \
  "ds_read_b32 v3, v2 offset:80\n\t" \
  "ds_read2_b32 v[108:109], %[h0] offset1:4\n\t" \
  "ds_read2_b32 v[110:111], %[h0] offset0:8 offset1:12\n\t" \
  "ds_read2_b32 v[120:121], %[h1] offset1:4\n\t" \
  "ds_read2_b32 v[112:113], v127 offset1:4\n\t" \
  "ds_read2_b32 v[114:115], v127 offset0:8 offset1:12\n\t" \
  "ds_read2_b32 v[116:117], v126 offset1:4\n\t" \
  "ds_read2_b32 v[118:119], v126 offset0:8 offset1:12\n\t" \
  "s_mov_b32 %[cn], 0x400000\n\t" \
  "v_add_u32 v104, v100, v122\n\t" \
  "v_add_u32 v105, v101, v122\n\t" \
  "v_add_u32 v106, v102, v122\n\t" \
  "v_add_u32 v107, v103, v122\n\t" \
  "v_and_or_b32 v104, v104, v123, %[tg]\n\t" \
  "v_and_or_b32 v105, v105, v123, %[tg]\n\t" \
  "v_and_or_b32 v106, v106, v123, %[tg]\n\t" \
  "v_and_or_b32 v107, v107, v123, %[tg]\n\t" \
  "buffer_store_dwordx4 v[104:107], %[ast], %[rs], 0 offen sc1\n\t" \
  ST_s12 \
  ".Ltwx:\n\t" \
  "s_waitcnt lgkmcnt(0)\n\t" \
  "v_cmp_gt_u32 vcc, %[pw2], v3\n\t" \
  "s_cbranch_vccnz .Ltwxs\n\t" \
  ".Ltwxg:\n\t" \
  "ds_read_b128 v[68:71], %[w2] offset:0\n\t" \
  "ds_read_b128 v[72:75], %[w2] offset:1024\n\t" \
  "ds_read_b128 v[76:79], %[w2] offset:2048\n\t" \
  "ds_read_b128 v[80:83], %[w2] offset:3072\n\t" \
  "ds_read_b128 v[84:87], %[w2] offset:4096\n\t" \
  "ds_read_b128 v[88:91], %[w2] offset:5120\n\t" \
  ".Ltcx:\n\t" \
  "v_xor_b32 v146, v108, %[tg]\n\t" \
  "v_xor_b32 v147, v109, %[tg]\n\t" \
  "v_xor_b32 v148, v110, %[tg]\n\t" \
  "v_xor_b32 v149, v111, %[tg]\n\t" \
  "v_xor_b32 v158, v120, %[tg]\n\t" \
  "v_xor_b32 v159, v121, %[tg]\n\t" \
  "v_pk_add_f32 v[132:133], v[146:147], v[148:149]\n\t" \
  "v_add_f32 v129, v158, v159\n\t" \
  "v_add_f32 v128, v132, v133\n\t" \
  "v_or3_b32 v138, v146, v147, v148\n\t" \
  "v_add_f32_dpp v129, v129, v129 quad_perm:[1,0,3,2] row_mask:0xf bank_mask:0xf\n\t" \
  "v_add_f32_dpp v128, v128, v128 quad_perm:[1,0,3,2] row_mask:0xf bank_mask:0xf\n\t" \
  "v_or3_b32 v139, v149, v158, v159\n\t" \
  "v_xor_b32 v150, v112, %[tg]\n\t" \
  "v_xor_b32 v151, v113, %[tg]\n\t" \
  "v_xor_b32 v152, v114, %[tg]\n\t" \
  "v_xor_b32 v153, v115, %[tg]\n\t" \
  "v_xor_b32 v154, v116, %[tg]\n\t" \
  "v_xor_b32 v155, v117, %[tg]\n\t" \
  "v_xor_b32 v156, v118, %[tg]\n\t" \
  "v_xor_b32 v157, v119, %[tg]\n\t" \
  "v_pk_add_f32 v[134:135], v[150:151], v[152:153]\n\t" \
  "v_pk_add_f32 v[136:137], v[154:155], v[156:157]\n\t" \
  "v_add_f32 v130, v134, v135\n\t" \
  "v_add_f32 v131, v136, v137\n\t" \
  "v_or3_b32 v138, v138, v150, v151\n\t" \
  "v_or3_b32 v139, v139, v152, v153\n\t" \
  "v_add_f32_dpp v130, v130, v130 quad_perm:[1,0,3,2] row_mask:0xf bank_mask:0xf\n\t" \
  "v_add_f32_dpp v131, v131, v131 quad_perm:[1,0,3,2] row_mask:0xf bank_mask:0xf\n\t" \
  "v_or3_b32 v138, v138, v154, v155\n\t" \
  "v_or3_b32 v139, v139, v156, v157\n\t" \
  "v_max_f32 v130, 0, v130\n\t" \
  "v_max_f32 v131, 0, v131\n\t" \
  "v_cvt_pk_bf16_f32 v130, v130, v130\n\t" \
  "v_cvt_pk_bf16_f32 v131, v131, v131\n\t" \
  "v_add_f32_dpp v129, v129, v129 quad_perm:[2,3,0,1] row_mask:0xf bank_mask:0xf\n\t" \
  "v_max_f32 v128, 0, v128\n\t" \
  "v_bitop3_b32 v138, v138, v139, v123 bitop3:0x54\n\t" \
  "v_cvt_pk_bf16_f32 v128, v128, v128\n\t" \
  "v_cmp_ne_u32 vcc, 0, v138\n\t" \
  "v_max_f32 v129, 0, v129\n\t" \
  "v_and_b32 v92, v130, %[k0]\n\t" \
  "v_and_b32_dpp v93, v130, %[k1] quad_perm:[2,3,0,1] row_mask:0xf bank_mask:0xf bound_ctrl:1\n\t" \
  "v_and_b32_dpp v94, v130, %[k2] row_shr:4 row_mask:0xf bank_mask:0xf bound_ctrl:1\n\t" \
  "v_and_b32_dpp v95, v130, %[k3] row_half_mirror row_mask:0xf bank_mask:0xf bound_ctrl:1\n\t" \
  "v_cvt_pk_bf16_f32 v129, v129, v129\n\t" \
  "s_cbranch_vccnz .Ltcxs\n\t" \
  ".Ltcxg:\n\t" \
  ST_s13 \
  "v_mfma_f32_16x16x32_bf16 v[100:103], v[92:95], v[36:39], 0\n\t" \
  "v_and_b32 v97, v130, %[k1]\n\t" \
  "v_and_b32_dpp v96, v130, %[k0] quad_perm:[2,3,0,1] row_mask:0xf bank_mask:0xf bound_ctrl:1\n\t" \
  "v_and_b32_dpp v99, v130, %[k3] row_shr:4 row_mask:0xf bank_mask:0xf bound_ctrl:1\n\t" \
  "v_and_b32_dpp v98, v130, %[k2] row_half_mirror row_mask:0xf bank_mask:0xf bound_ctrl:1\n\t" \
  "s_nop 0\n\t" \
  "s_nop 0\n\t" \
  "v_mfma_f32_16x16x32_bf16 v[100:103], v[96:99], v[40:43], v[100:103]\n\t" \
  "v_and_b32 v94, v130, %[k2]\n\t" \
  "v_and_b32_dpp v95, v130, %[k3] quad_perm:[2,3,0,1] row_mask:0xf bank_mask:0xf bound_ctrl:1\n\t" \
  "v_and_b32_dpp v92, v130, %[k0] row_shl:4 row_mask:0xf bank_mask:0xf bound_ctrl:1\n\t" \
  "v_and_b32_dpp v93, v130, %[k1] row_half_mirror row_mask:0xf bank_mask:0xf bound_ctrl:1\n\t" \
  "s_nop 0\n\t" \
  "s_nop 0\n\t" \
  "v_mfma_f32_16x16x32_bf16 v[100:103], v[92:95], v[44:47], v[100:103]\n\t" \
  "v_and_b32 v99, v130, %[k3]\n\t" \
  "v_and_b32_dpp v98, v130, %[k2] quad_perm:[2,3,0,1] row_mask:0xf bank_mask:0xf bound_ctrl:1\n\t" \
  "v_and_b32_dpp v97, v130, %[k1] row_shl:4 row_mask:0xf bank_mask:0xf bound_ctrl:1\n\t" \
  "v_and_b32_dpp v96, v130, %[k0] row_half_mirror row_mask:0xf bank_mask:0xf bound_ctrl:1\n\t" \
  "s_nop 0\n\t" \
  "s_nop 0\n\t" \
  "v_mfma_f32_16x16x32_bf16 v[100:103], v[96:99], v[48:51], v[100:103]\n\t" \
  "v_and_b32 v92, v131, %[k0]\n\t" \
  "v_and_b32_dpp v93, v131, %[k1] quad_perm:[2,3,0,1] row_mask:0xf bank_mask:0xf bound_ctrl:1\n\t" \
  "v_and_b32_dpp v94, v131, %[k2] row_shr:4 row_mask:0xf bank_mask:0xf bound_ctrl:1\n\t" \
  "v_and_b32_dpp v95, v131, %[k3] row_half_mirror row_mask:0xf bank_mask:0xf bound_ctrl:1\n\t" \
  "s_nop 0\n\t" \
  "s_nop 0\n\t" \
  "v_mfma_f32_16x16x32_bf16 v[100:103], v[92:95], v[52:55], v[100:103]\n\t" \
  "v_and_b32 v97, v131, %[k1]\n\t" \
  "v_and_b32_dpp v96, v131, %[k0] quad_perm:[2,3,0,1] row_mask:0xf bank_mask:0xf bound_ctrl:1\n\t" \
  "v_and_b32_dpp v99, v131, %[k3] row_shr:4 row_mask:0xf bank_mask:0xf bound_ctrl:1\n\t" \
  "v_and_b32_dpp v98, v131, %[k2] row_half_mirror row_mask:0xf bank_mask:0xf bound_ctrl:1\n\t" \
  "s_nop 0\n\t" \
  "s_nop 0\n\t" \
  "v_mfma_f32_16x16x32_bf16 v[100:103], v[96:99], v[56:59], v[100:103]\n\t" \
  "v_and_b32 v94, v131, %[k2]\n\t" \
  "v_and_b32_dpp v95, v131, %[k3] quad_perm:[2,3,0,1] row_mask:0xf bank_mask:0xf bound_ctrl:1\n\t" \
  "v_and_b32_dpp v92, v131, %[k0] row_shl:4 row_mask:0xf bank_mask:0xf bound_ctrl:1\n\t" \
  "v_and_b32_dpp v93, v131, %[k1] row_half_mirror row_mask:0xf bank_mask:0xf bound_ctrl:1\n\t" \
  "s_nop 0\n\t" \
  "s_nop 0\n\t" \
  "v_mfma_f32_16x16x32_bf16 v[100:103], v[92:95], v[60:63], v[100:103]\n\t" \
  "v_and_b32 v99, v131, %[k3]\n\t" \
  "v_and_b32_dpp v98, v131, %[k2] quad_perm:[2,3,0,1] row_mask:0xf bank_mask:0xf bound_ctrl:1\n\t" \
  "v_and_b32_dpp v97, v131, %[k1] row_shl:4 row_mask:0xf bank_mask:0xf bound_ctrl:1\n\t" \
  "v_and_b32_dpp v96, v131, %[k0] row_half_mirror row_mask:0xf bank_mask:0xf bound_ctrl:1\n\t" \
  "s_nop 0\n\t" \
  "s_nop 0\n\t" \
  "v_mfma_f32_16x16x32_bf16 v[100:103], v[96:99], v[64:67], v[100:103]\n\t" \
  "v_and_b32 v92, v128, %[k0]\n\t" \
  "v_and_b32_dpp v93, v128, %[k1] quad_perm:[2,3,0,1] row_mask:0xf bank_mask:0xf bound_ctrl:1\n\t" \
  "v_and_b32_dpp v94, v128, %[k2] row_shr:4 row_mask:0xf bank_mask:0xf bound_ctrl:1\n\t" \
  "v_and_b32_dpp v95, v128, %[k3] row_half_mirror row_mask:0xf bank_mask:0xf bound_ctrl:1\n\t" \
  "s_waitcnt lgkmcnt(5)\n\t" \
  "s_nop 0\n\t" \
  "v_mfma_f32_16x16x32_bf16 v[100:103], v[92:95], v[68:71], v[100:103]\n\t" \
  "v_and_b32 v97, v128, %[k1]\n\t" \
  "v_and_b32_dpp v96, v128, %[k0] quad_perm:[2,3,0,1] row_mask:0xf bank_mask:0xf bound_ctrl:1\n\t" \
  "v_and_b32_dpp v99, v128, %[k3] row_shr:4 row_mask:0xf bank_mask:0xf bound_ctrl:1\n\t" \
  "v_and_b32_dpp v98, v128, %[k2] row_half_mirror row_mask:0xf bank_mask:0xf bound_ctrl:1\n\t" \
  "s_waitcnt lgkmcnt(4)\n\t" \
  "s_nop 0\n\t" \
  "v_mfma_f32_16x16x32_bf16 v[100:103], v[96:99], v[72:75], v[100:103]\n\t" \
  "v_and_b32 v94, v128, %[k2]\n\t" \
  "v_and_b32_dpp v95, v128, %[k3] quad_perm:[2,3,0,1] row_mask:0xf bank_mask:0xf bound_ctrl:1\n\t" \
  "v_and_b32_dpp v92, v128, %[k0] row_shl:4 row_mask:0xf bank_mask:0xf bound_ctrl:1\n\t" \
  "v_and_b32_dpp v93, v128, %[k1] row_half_mirror row_mask:0xf bank_mask:0xf bound_ctrl:1\n\t" \
  "s_waitcnt lgkmcnt(3)\n\t" \
  "s_nop 0\n\t" \
  "v_mfma_f32_16x16x32_bf16 v[100:103], v[92:95], v[76:79], v[100:103]\n\t" \
  "v_and_b32 v99, v128, %[k3]\n\t" \
  "v_and_b32_dpp v98, v128, %[k2] quad_perm:[2,3,0,1] row_mask:0xf bank_mask:0xf bound_ctrl:1\n\t" \
  "v_and_b32_dpp v97, v128, %[k1] row_shl:4 row_mask:0xf bank_mask:0xf bound_ctrl:1\n\t" \
  "v_and_b32_dpp v96, v128, %[k0] row_half_mirror row_mask:0xf bank_mask:0xf bound_ctrl:1\n\t" \
  "s_waitcnt lgkmcnt(2)\n\t" \
  "s_nop 0\n\t" \
  "v_mfma_f32_16x16x32_bf16 v[100:103], v[96:99], v[80:83], v[100:103]\n\t" \
  "v_and_b32 v92, v129, %[k0]\n\t" \
  "v_and_b32 v93, v129, %[k1]\n\t" \
  "v_and_b32_dpp v94, v129, %[k2] row_shr:4 row_mask:0xf bank_mask:0xf bound_ctrl:1\n\t" \
  "v_and_b32_dpp v95, v129, %[k3] row_shr:4 row_mask:0xf bank_mask:0xf bound_ctrl:1\n\t" \
  "s_waitcnt lgkmcnt(1)\n\t" \
  "s_nop 0\n\t" \
  "v_mfma_f32_16x16x32_bf16 v[100:103], v[92:95], v[84:87], v[100:103]\n\t" \
  "v_and_b32 v98, v129, %[k2]\n\t" \
  "v_and_b32 v99, v129, %[k3]\n\t" \
  "v_and_b32_dpp v96, v129, %[k0] row_shl:4 row_mask:0xf bank_mask:0xf bound_ctrl:1\n\t" \
  "v_and_b32_dpp v97, v129, %[k1] row_shl:4 row_mask:0xf bank_mask:0xf bound_ctrl:1\n\t" \
  "s_waitcnt lgkmcnt(0)\n\t" \
  "s_nop 0\n\t" \
  "v_mfma_f32_16x16x32_bf16 v[100:103], v[96:99], v[88:91], v[100:103]\n\t" \
  "s_nop 7\n\t" \
  "v_add_u32 v104, v100, v122\n\t" \
  "v_add_u32 v105, v101, v122\n\t" \
  "v_add_u32 v106, v102, v122\n\t" \
  "v_add_u32 v107, v103, v122\n\t" \
  "v_and_or_b32 v104, v104, v123, %[tg]\n\t" \
  "v_and_or_b32 v105, v105, v123, %[tg]\n\t" \
  "v_and_or_b32 v106, v106, v123, %[tg]\n\t" \
  "v_and_or_b32 v107, v107, v123, %[tg]\n\t" \
  "buffer_store_dwordx4 v[104:107], %[ast], %[rs], %[t1] offen sc1\n\t" \
  "ds_write_b32 %[fl], v125\n\t" \
  "s_cbranch_execz .Lta0\n\t" \
  "s_branch .Ltend\n\t" \
  ".Ltwxs:\n\t" \
  "ds_read_b32 v3, v2 offset:80\n\t" \
  "ds_read2_b32 v[108:109], %[h0] offset1:4\n\t" \
  "ds_read2_b32 v[110:111], %[h0] offset0:8 offset1:12\n\t" \
  "ds_read2_b32 v[120:121], %[h1] offset1:4\n\t" \
  "ds_read2_b32 v[112:113], v127 offset1:4\n\t" \
  "ds_read2_b32 v[114:115], v127 offset0:8 offset1:12\n\t" \
  "ds_read2_b32 v[116:117], v126 offset1:4\n\t" \
  "ds_read2_b32 v[118:119], v126 offset0:8 offset1:12\n\t" \
  "s_sub_u32 %[cn], %[cn], 1\n\t" \
  "s_cbranch_scc0 .Ltwx\n\t" \
  "s_mov_b32 %[cn], 0\n\t" \
  "s_branch .Ltwxg\n\t" \
  ".Ltcxs:\n\t" \
  "v_cmp_gt_u32 vcc, %[ph], v3\n\t" \
  "s_cbranch_vccz .Lthx\n\t" \
  "ds_read_b32 v3, v2 offset:80\n\t" \
  "ds_read2_b32 v[108:109], %[h0] offset1:4\n\t" \
  "ds_read2_b32 v[110:111], %[h0] offset0:8 offset1:12\n\t" \
  "ds_read2_b32 v[120:121], %[h1] offset1:4\n\t" \
  "ds_read2_b32 v[112:113], v127 offset1:4\n\t" \
  "ds_read2_b32 v[114:115], v127 offset0:8 offset1:12\n\t" \
  "ds_read2_b32 v[116:117], v126 offset1:4\n\t" \
  "ds_read2_b32 v[118:119], v126 offset0:8 offset1:12\n\t" \
  "s_waitcnt lgkmcnt(0)\n\t" \
  "s_sub_u32 %[cn], %[cn], 1\n\t" \
  "s_cbranch_scc0 .Ltcx\n\t" \
  "s_mov_b32 %[cn], 0\n\t" \
  "s_branch .Ltcxg\n\t" \
  ".Lthx:\n\t" \
  "buffer_load_dword v108, %[ah0], %[rs], 0 offen offset:0 sc1\n\t" \
  "buffer_load_dword v109, %[ah0], %[rs], 0 offen offset:128 sc1\n\t" \
  "buffer_load_dword v110, %[ah0], %[rs], 0 offen offset:256 sc1\n\t" \
  "buffer_load_dword v111, %[ah0], %[rs], 0 offen offset:384 sc1\n\t" \
  "buffer_load_dword v120, %[ah1], %[rs], 0 offen offset:0 sc1\n\t" \
  "buffer_load_dword v121, %[ah1], %[rs], 0 offen offset:128 sc1\n\t" \
  "buffer_load_dword v112, %[ahx], %[rs], 0 offen offset:0 sc1\n\t" \
  "buffer_load_dword v113, %[ahx], %[rs], 0 offen offset:128 sc1\n\t" \
  "buffer_load_dword v114, %[ahx], %[rs], 0 offen offset:256 sc1\n\t" \
  "buffer_load_dword v115, %[ahx], %[rs], 0 offen offset:384 sc1\n\t" \
  "buffer_load_dword v116, %[ahx], %[rs], 0 offen offset:16 sc1\n\t" \
  "buffer_load_dword v117, %[ahx], %[rs], 0 offen offset:144 sc1\n\t" \
  "buffer_load_dword v118, %[ahx], %[rs], 0 offen offset:272 sc1\n\t" \
  "buffer_load_dword v119, %[ahx], %[rs], 0 offen offset:400 sc1\n\t" \
  "s_waitcnt vmcnt(0)\n\t" \
  "s_sub_u32 %[cn], %[cn], 64\n\t" \
  "s_cbranch_scc0 .Ltcx\n\t" \
  "s_mov_b32 %[cn], 0\n\t" \
  "s_branch .Ltcxg\n\t" \
  ".Ltend:\n\t" \
  ST_wait \
  ""
#define TAIL_CLOB "v2", "v3", "v4", "v5", "v6", "v7", "v8", "v9", "v10", "v11", "v12", "v13", "v14", "v15", "v16", "v17", "v18", "v19", "v20", "v21", "v22", "v23", "v24", "v25", "v26", "v27", "v28", "v29", "v30", "v31", "v32", "v33", "v34", "v35", "v36", "v37", "v38", "v39", "v40", "v41", "v42", "v43", "v44", "v45", "v46", "v47", "v48", "v49", "v50", "v51", "v52", "v53", "v54", "v55", "v56", "v57", "v58", "v59", "v60", "v61", "v62", "v63", "v64", "v65", "v66", "v67", "v68", "v69", "v70", "v71", "v72", "v73", "v74", "v75", "v76", "v77", "v78", "v79", "v80", "v81", "v82", "v83", "v84", "v85", "v86", "v87", "v88", "v89", "v90", "v91", "v92", "v93", "v94", "v95", "v96", "v97", "v98", "v99", "v100", "v101", "v102", "v103", "v104", "v105", "v106", "v107", "v108", "v109", "v110", "v111", "v112", "v113", "v114", "v115", "v116", "v117", "v118", "v119", "v120", "v121", "v122", "v123", "v124", "v125", "v126", "v127", "v128", "v129", "v130", "v131", "v132", "v133", "v134", "v135", "v136", "v137", "v138", "v139", "v140", "v141", "v142", "v143", "v144", "v145", "v146", "v147", "v148", "v149", "v150", "v151", "v152", "v153", "v154", "v155", "v156", "v157", "v158", "v159"
#define FIN \
  "v_mov_b32 v116, %[ls]\n\t" \
  "v_mov_b32 v117, 0xfffff000\n\t" \
  "s_mov_b32 %[cn], 0x400000\n\t" \
  ".Lf0a:\n\t" \
  "ds_read_b128 v[112:115], v116 offset:64\n\t" \
  "s_waitcnt lgkmcnt(0)\n\t" \
  "v_min3_u32 v96, v112, v113, v114\n\t" \
  "v_min_u32 v96, v96, v115\n\t" \
  "v_cmp_gt_u32 vcc, %[fs1], v96\n\t" \
  "s_cbranch_vccz .Lf0ag\n\t" \
  "s_sleep 1\n\t" \
  "s_sub_u32 %[cn], %[cn], 1\n\t" \
  "s_cbranch_scc0 .Lf0a\n\t" \
  "s_mov_b32 %[cn], 0\n\t" \
  ".Lf0ag:\n\t" \
  "s_sleep %[fd1]\n\t" \
  "s_mov_b32 %[t0], 0x0\n\t" \
  "buffer_load_dwordx4 v[64:67], %[va], %[rs], %[t0] offen sc1\n\t" \
  "s_mov_b32 %[t0], 0x40000\n\t" \
  "buffer_load_dwordx4 v[68:71], %[va], %[rs], %[t0] offen sc1\n\t" \
  "s_mov_b32 %[t0], 0x80000\n\t" \
  "buffer_load_dwordx4 v[72:75], %[va], %[rs], %[t0] offen sc1\n\t" \
  "s_mov_b32 %[t0], 0xc0000\n\t" \
  "buffer_load_dwordx4 v[76:79], %[va], %[rs], %[t0] offen sc1\n\t" \
  "s_mov_b32 %[cn], 0x400000\n\t" \
  ".Lf0b:\n\t" \
  "ds_read_b128 v[112:115], v116 offset:64\n\t" \
  "s_waitcnt lgkmcnt(0)\n\t" \
  "v_min3_u32 v96, v112, v113, v114\n\t" \
  "v_min_u32 v96, v96, v115\n\t" \
  "v_cmp_gt_u32 vcc, %[fs], v96\n\t" \
  "s_cbranch_vccz .Lf0bg\n\t" \
  "s_sleep 1\n\t" \
  "s_sub_u32 %[cn], %[cn], 1\n\t" \
  "s_cbranch_scc0 .Lf0b\n\t" \
  "s_mov_b32 %[cn], 0\n\t" \
  ".Lf0bg:\n\t" \
  "s_sleep %[fd]\n\t" \
  ST_s7 \
  "s_mov_b32 %[t0], 0x100000\n\t" \
  "buffer_load_dwordx4 v[80:83], %[va], %[rs], %[t0] offen sc1\n\t" \
  "s_mov_b32 %[t0], 0x140000\n\t" \
  "buffer_load_dwordx4 v[84:87], %[va], %[rs], %[t0] offen sc1\n\t" \
  "s_mov_b32 %[t0], 0x180000\n\t" \
  "buffer_load_dwordx4 v[88:91], %[va], %[rs], %[t0] offen sc1\n\t" \
  "s_mov_b32 %[t0], 0x1c0000\n\t" \
  "buffer_load_dwordx4 v[92:95], %[va], %[rs], %[t0] offen sc1\n\t" \
  "s_mov_b32 %[cn], 0x40000\n\t" \
  ".Lf2:\n\t" \
  "s_waitcnt vmcnt(7)\n\t" \
  "v_xor_b32 v120, v64, %[tg]\n\t" \
  "v_xor_b32 v121, v65, %[tg]\n\t" \
  "v_xor_b32 v122, v66, %[tg]\n\t" \
  "v_xor_b32 v123, v67, %[tg]\n\t" \
  "s_waitcnt vmcnt(6)\n\t" \
  "v_xor_b32 v124, v68, %[tg]\n\t" \
  "v_xor_b32 v125, v69, %[tg]\n\t" \
  "v_xor_b32 v126, v70, %[tg]\n\t" \
  "v_xor_b32 v127, v71, %[tg]\n\t" \
  "v_pk_add_f32 v[104:105], v[120:121], v[124:125]\n\t" \
  "v_pk_add_f32 v[106:107], v[122:123], v[126:127]\n\t" \
  "s_waitcnt vmcnt(5)\n\t" \
  "v_xor_b32 v128, v72, %[tg]\n\t" \
  "v_xor_b32 v129, v73, %[tg]\n\t" \
  "v_xor_b32 v130, v74, %[tg]\n\t" \
  "v_xor_b32 v131, v75, %[tg]\n\t" \
  "v_pk_add_f32 v[104:105], v[104:105], v[128:129]\n\t" \
  "v_pk_add_f32 v[106:107], v[106:107], v[130:131]\n\t" \
  "s_waitcnt vmcnt(4)\n\t" \
  "v_xor_b32 v132, v76, %[tg]\n\t" \
  "v_xor_b32 v133, v77, %[tg]\n\t" \
  "v_xor_b32 v134, v78, %[tg]\n\t" \
  "v_xor_b32 v135, v79, %[tg]\n\t" \
  "v_pk_add_f32 v[104:105], v[104:105], v[132:133]\n\t" \
  "v_pk_add_f32 v[106:107], v[106:107], v[134:135]\n\t" \
  "v_or3_b32 v96, v120, v121, v122\n\t" \
  "v_bitop3_b32 v96, v96, v123, v117 bitop3:0x54\n\t" \
  "v_permlane32_swap_b32 v104, v106\n\t" \
  "v_permlane32_swap_b32 v105, v107\n\t" \
  "v_add_f32 v104, v104, v106\n\t" \
  "v_add_f32 v105, v105, v107\n\t" \
  "v_or3_b32 v98, v124, v125, v126\n\t" \
  "v_bitop3_b32 v98, v98, v127, v117 bitop3:0x54\n\t" \
  "v_permlane16_swap_b32 v104, v105\n\t" \
  "v_add_f32 v104, v104, v105\n\t" \
  "v_or3_b32 v100, v128, v129, v130\n\t" \
  "v_bitop3_b32 v100, v100, v131, v117 bitop3:0x54\n\t" \
  "v_add_f32_dpp v104, v104, v104 quad_perm:[2,3,0,1] row_mask:0xf bank_mask:0xf\n\t" \
  "v_or3_b32 v102, v132, v133, v134\n\t" \
  "v_bitop3_b32 v102, v102, v135, v117 bitop3:0x54\n\t" \
  "v_add_f32_dpp v104, v104, v104 row_ror:4 row_mask:0xf bank_mask:0xf\n\t" \
  "v_or3_b32 v108, v96, v98, v100\n\t" \
  "v_or_b32 v108, v108, v102\n\t" \
  "v_add_f32_dpp v104, v104, v104 row_ror:8 row_mask:0xf bank_mask:0xf\n\t" \
  "v_cmp_ne_u32 vcc, 0, v108\n\t" \
  "v_cvt_pk_bf16_f32 v118, 0, v104\n\t" \
  "s_cbranch_vccnz .Lf2s\n\t" \
  ".Lf3:\n\t" \
  "s_waitcnt vmcnt(3)\n\t" \
  "v_xor_b32 v120, v80, %[tg]\n\t" \
  "v_xor_b32 v121, v81, %[tg]\n\t" \
  "v_xor_b32 v122, v82, %[tg]\n\t" \
  "v_xor_b32 v123, v83, %[tg]\n\t" \
  "s_waitcnt vmcnt(2)\n\t" \
  "v_xor_b32 v124, v84, %[tg]\n\t" \
  "v_xor_b32 v125, v85, %[tg]\n\t" \
  "v_xor_b32 v126, v86, %[tg]\n\t" \
  "v_xor_b32 v127, v87, %[tg]\n\t" \
  "v_pk_add_f32 v[104:105], v[120:121], v[124:125]\n\t" \
  "v_pk_add_f32 v[106:107], v[122:123], v[126:127]\n\t" \
  "s_waitcnt vmcnt(1)\n\t" \
  "v_xor_b32 v128, v88, %[tg]\n\t" \
  "v_xor_b32 v129, v89, %[tg]\n\t" \
  "v_xor_b32 v130, v90, %[tg]\n\t" \
  "v_xor_b32 v131, v91, %[tg]\n\t" \
  "v_pk_add_f32 v[104:105], v[104:105], v[128:129]\n\t" \
  "v_pk_add_f32 v[106:107], v[106:107], v[130:131]\n\t" \
  "s_waitcnt vmcnt(0)\n\t" \
  ST_s1 \
  "v_xor_b32 v132, v92, %[tg]\n\t" \
  "v_xor_b32 v133, v93, %[tg]\n\t" \
  "v_xor_b32 v134, v94, %[tg]\n\t" \
  "v_xor_b32 v135, v95, %[tg]\n\t" \
  "v_pk_add_f32 v[104:105], v[104:105], v[132:133]\n\t" \
  "v_pk_add_f32 v[106:107], v[106:107], v[134:135]\n\t" \
  "v_or3_b32 v96, v120, v121, v122\n\t" \
  "v_bitop3_b32 v96, v96, v123, v117 bitop3:0x54\n\t" \
  "v_permlane32_swap_b32 v104, v106\n\t" \
  "v_permlane32_swap_b32 v105, v107\n\t" \
  "v_add_f32 v104, v104, v106\n\t" \
  "v_add_f32 v105, v105, v107\n\t" \
  "v_or3_b32 v98, v124, v125, v126\n\t" \
  "v_bitop3_b32 v98, v98, v127, v117 bitop3:0x54\n\t" \
  "v_permlane16_swap_b32 v104, v105\n\t" \
  "v_add_f32 v104, v104, v105\n\t" \
  "v_or3_b32 v100, v128, v129, v130\n\t" \
  "v_bitop3_b32 v100, v100, v131, v117 bitop3:0x54\n\t" \
  "v_add_f32_dpp v104, v104, v104 quad_perm:[2,3,0,1] row_mask:0xf bank_mask:0xf\n\t" \
  "v_or3_b32 v102, v132, v133, v134\n\t" \
  "v_bitop3_b32 v102, v102, v135, v117 bitop3:0x54\n\t" \
  "v_add_f32_dpp v104, v104, v104 row_ror:4 row_mask:0xf bank_mask:0xf\n\t" \
  "v_or3_b32 v108, v96, v98, v100\n\t" \
  "v_or_b32 v108, v108, v102\n\t" \
  "v_add_f32_dpp v104, v104, v104 row_ror:8 row_mask:0xf bank_mask:0xf\n\t" \
  "v_cmp_ne_u32 vcc, 0, v108\n\t" \
  "v_cvt_pk_bf16_f32 v104, 0, v104\n\t" \
  "v_mul_f32 v119, 0xbfb8aa3b, v104\n\t" \
  "v_exp_f32 v119, v119\n\t" \
  "s_cbranch_vccnz .Lf3s\n\t" \
  ".Lf4:\n\t" \
  "v_add_f32 v119, 1.0, v119\n\t" \
  "v_rcp_f32 v119, v119\n\t" \
  "s_and_b32 exec_lo, exec_lo, 0x30003\n\t" \
  "v_mul_f32 v119, v104, v119\n\t" \
  "v_cvt_pk_bf16_f32 v119, 0, v119\n\t" \
  "v_mul_f32 v119, v118, v119\n\t" \
  "v_cvt_pk_bf16_f32 v119, 0, v119\n\t" \
  "v_add_f32 v119, %[xf], v119\n\t" \
  "s_and_b32 exec_hi, exec_hi, 0x30003\n\t" \
  "v_cvt_pk_bf16_f32 v119, v119, v119\n\t" \
  "global_store_short %[oa], v119, %[out]\n\t" \
  "buffer_store_dword %[tg], %[ea], %[rs], 0 offen\n\t" \
  "s_branch .Lfend\n\t" \
  ".Lf2s:\n\t" \
  "v_cmp_ne_u32 vcc, 0, v96\n\t" \
  "s_mov_b64 exec, vcc\n\t" \
  "s_mov_b32 %[t0], 0x0\n\t" \
  "buffer_load_dwordx4 v[64:67], %[va], %[rs], %[t0] offen sc1\n\t" \
  "s_mov_b64 exec, -1\n\t" \
  "v_cmp_ne_u32 vcc, 0, v98\n\t" \
  "s_mov_b64 exec, vcc\n\t" \
  "s_mov_b32 %[t0], 0x40000\n\t" \
  "buffer_load_dwordx4 v[68:71], %[va], %[rs], %[t0] offen sc1\n\t" \
  "s_mov_b64 exec, -1\n\t" \
  "v_cmp_ne_u32 vcc, 0, v100\n\t" \
  "s_mov_b64 exec, vcc\n\t" \
  "s_mov_b32 %[t0], 0x80000\n\t" \
  "buffer_load_dwordx4 v[72:75], %[va], %[rs], %[t0] offen sc1\n\t" \
  "s_mov_b64 exec, -1\n\t" \
  "v_cmp_ne_u32 vcc, 0, v102\n\t" \
  "s_mov_b64 exec, vcc\n\t" \
  "s_mov_b32 %[t0], 0xc0000\n\t" \
  "buffer_load_dwordx4 v[76:79], %[va], %[rs], %[t0] offen sc1\n\t" \
  "s_mov_b64 exec, -1\n\t" \
  "s_waitcnt vmcnt(4)\n\t" \
  "v_bitop3_b32 v96, v80, v81, %[tg] bitop3:0x7e\n\t" \
  "v_bitop3_b32 v97, v82, v83, %[tg] bitop3:0x7e\n\t" \
  "v_bitop3_b32 v98, v84, v85, %[tg] bitop3:0x7e\n\t" \
  "v_bitop3_b32 v99, v86, v87, %[tg] bitop3:0x7e\n\t" \
  "v_bitop3_b32 v100, v88, v89, %[tg] bitop3:0x7e\n\t" \
  "v_bitop3_b32 v101, v90, v91, %[tg] bitop3:0x7e\n\t" \
  "v_bitop3_b32 v102, v92, v93, %[tg] bitop3:0x7e\n\t" \
  "v_bitop3_b32 v103, v94, v95, %[tg] bitop3:0x7e\n\t" \
  "v_bitop3_b32 v96, v96, v97, v117 bitop3:0x54\n\t" \
  "v_bitop3_b32 v98, v98, v99, v117 bitop3:0x54\n\t" \
  "v_bitop3_b32 v100, v100, v101, v117 bitop3:0x54\n\t" \
  "v_bitop3_b32 v102, v102, v103, v117 bitop3:0x54\n\t" \
  "v_cmp_ne_u32 vcc, 0, v96\n\t" \
  "s_mov_b64 exec, vcc\n\t" \
  "s_mov_b32 %[t0], 0x100000\n\t" \
  "buffer_load_dwordx4 v[80:83], %[va], %[rs], %[t0] offen sc1\n\t" \
  "s_mov_b64 exec, -1\n\t" \
  "v_cmp_ne_u32 vcc, 0, v98\n\t" \
  "s_mov_b64 exec, vcc\n\t" \
  "s_mov_b32 %[t0], 0x140000\n\t" \
  "buffer_load_dwordx4 v[84:87], %[va], %[rs], %[t0] offen sc1\n\t" \
  "s_mov_b64 exec, -1\n\t" \
  "v_cmp_ne_u32 vcc, 0, v100\n\t" \
  "s_mov_b64 exec, vcc\n\t" \
  "s_mov_b32 %[t0], 0x180000\n\t" \
  "buffer_load_dwordx4 v[88:91], %[va], %[rs], %[t0] offen sc1\n\t" \
  "s_mov_b64 exec, -1\n\t" \
  "v_cmp_ne_u32 vcc, 0, v102\n\t" \
  "s_mov_b64 exec, vcc\n\t" \
  "s_mov_b32 %[t0], 0x1c0000\n\t" \
  "buffer_load_dwordx4 v[92:95], %[va], %[rs], %[t0] offen sc1\n\t" \
  "s_mov_b64 exec, -1\n\t" \
  "s_sub_u32 %[cn], %[cn], 1\n\t" \
  "s_cbranch_scc0 .Lf2\n\t" \
  "s_mov_b32 %[cn], 0\n\t" \
  "s_branch .Lf3\n\t" \
  ".Lf3s:\n\t" \
  "v_cmp_ne_u32 vcc, 0, v96\n\t" \
  "s_mov_b64 exec, vcc\n\t" \
  "s_mov_b32 %[t0], 0x100000\n\t" \
  "buffer_load_dwordx4 v[80:83], %[va], %[rs], %[t0] offen sc1\n\t" \
  "s_mov_b64 exec, -1\n\t" \
  "v_cmp_ne_u32 vcc, 0, v98\n\t" \
  "s_mov_b64 exec, vcc\n\t" \
  "s_mov_b32 %[t0], 0x140000\n\t" \
  "buffer_load_dwordx4 v[84:87], %[va], %[rs], %[t0] offen sc1\n\t" \
  "s_mov_b64 exec, -1\n\t" \
  "v_cmp_ne_u32 vcc, 0, v100\n\t" \
  "s_mov_b64 exec, vcc\n\t" \
  "s_mov_b32 %[t0], 0x180000\n\t" \
  "buffer_load_dwordx4 v[88:91], %[va], %[rs], %[t0] offen sc1\n\t" \
  "s_mov_b64 exec, -1\n\t" \
  "v_cmp_ne_u32 vcc, 0, v102\n\t" \
  "s_mov_b64 exec, vcc\n\t" \
  "s_mov_b32 %[t0], 0x1c0000\n\t" \
  "buffer_load_dwordx4 v[92:95], %[va], %[rs], %[t0] offen sc1\n\t" \
  "s_mov_b64 exec, -1\n\t" \
  "s_sub_u32 %[cn], %[cn], 1\n\t" \
  "s_cbranch_scc0 .Lf3\n\t" \
  "s_mov_b32 %[cn], 0\n\t" \
  "s_waitcnt vmcnt(0)\n\t" \
  "s_branch .Lf4\n\t" \
  ".Lfend:\n\t" \
  "s_mov_b64 exec, -1\n\t" \
  ST_wait \
  ""
#define FIN_CLOB "v64", "v65", "v66", "v67", "v68", "v69", "v70", "v71", "v72", "v73", "v74", "v75", "v76", "v77", "v78", "v79", "v80", "v81", "v82", "v83", "v84", "v85", "v86", "v87", "v88", "v89", "v90", "v91", "v92", "v93", "v94", "v95", "v96", "v97", "v98", "v99", "v100", "v101", "v102", "v103", "v104", "v105", "v106", "v107", "v108", "v109", "v110", "v111", "v112", "v113", "v114", "v115", "v116", "v117", "v118", "v119", "v120", "v121", "v122", "v123", "v124", "v125", "v126", "v127", "v128", "v129", "v130", "v131", "v132", "v133", "v134", "v135"
DI void cpf(u32 tid) {
  u32x4 r;
  u32 v;
  asm volatile("s_getpc_b64 %0\n.Lcode0:\n\ts_mov_b32 m0, %2\n\tv_lshlrev_b32 %1, 7, %3" : "=s"(*(u64*)&r), "=v"(v) : "n"(L_END), "v"(tid));
  asm volatile("s_mov_b32 %0, .Lcode1-.Lcode0" : "=s"(r[2]));
  r[3] = 0x00020000u;
  asm volatile("buffer_load_dword %0, %1, 0 offen lds" ::"v"(v), "s"(r) : "memory");
}
__global__ __launch_bounds__(320, 2) void fused_kernel(const void* __restrict__ x, const void* __restrict__ wq, const void* __restrict__ kc,
                                                       void* ws, const void* __restrict__ vc, const void* __restrict__ w1,
                                                       const void* __restrict__ wo, const void* __restrict__ w2, u32* __restrict__ out) {
  u64* stamp = nullptr;
  STAMP(0);
  cpf(threadIdx.x * CD + ((blockIdx.x >> 3) & (CD - 1)));
  const u32 bid = blockIdx.x;
  const u32 xcd = bid & 7, jj = (bid >> 3) ^ JX, p = xcd * 32 + jj;
  const u32 tid = threadIdx.x, lane = tid & 63, g = lane >> 4, n = lane & 15;
  const u32 w = __builtin_amdgcn_readfirstlane(tid >> 6);
  const u32 hkv = xcd >> 1;
  const bool has1 = jj < 28;
  const u32 qd = (p >> 1) & 3, ch = p & 1, rb = p >> 1, ex = xcd * 2 + ((jj >> 1) & 1);
  const u32 ho = (p >> 3) ^ HX;
  auto hq = [&](u32 Q) { return HP_OFF + (Q >> 2) * 1024 + (n & 7) * 128 + (n >> 3) * 64 + (Q & 3) * 16; };
  u32 vxs = tid * 16, vxa = (n >> 3) * 2048 + g * 16, vq = g * 65536 + n * 16;
  u32 sxa = xcd * 256 + w * 64, sq = (xcd * 128 + w * 32) * 8192 + (jj ^ QX) * 256;
  u32 kvb = (((xcd & 1) * 4 + hkv) * 32768 + 28672) * 256u + (jj ^ KX) * KJS;
  u32 vk = (4 * (n & 3) + ((n >> 2) & 1)) * 256 + (g + 4 * (n >> 3)) * 16;
  keep(vxs); keep(vxa); keep(vq); keep(vk);
  asm volatile("" : "+s"(sxa), "+s"(sq), "+s"(kvb));
  asm volatile("" ::"s"(x), "s"(wq), "s"(kc), "s"(ws), "s"(vc), "s"(w1), "s"(wo), "s"(w2), "s"(out));
  if (w == 4) {
    __builtin_amdgcn_s_setprio(3);
    Ld l;
    l.vk = vk;
    l.rk = mks(kc, kvb, 3 * KWS + 8192);
    issr<0, 1>(l);
    l.t0 = (u32)__builtin_readsteadycounter();
    issr<1, E1>(l);
    STAMP(9);
    l.vv = lane * 16;
    l.v1 = g * (has1 ? 57344 : 16384) + n * 16;
    l.vo = g * 65536 + n * 16;
    l.v2a = g * 8192 + n * 16;
    l.v2b = 32768 + g * 4096 + n * 16;
    l.sa = has1 ? 32 * 7168 : 256;
    l.sb = has1 ? 7168 : 2048;
    l.rv = mks(vc, kvb, 3 * KWS + 8192);
    l.r1 = mks(has1 ? w1 : w2, has1 ? xcd * 128 * 7168 + jj * 256 : (3072 + ex * 32) * 2048 + ch * 1024, has1 ? 127 * 7168 + 256 : 31 * 2048 + 1024);
    l.ro = mks(wo, (ho * 128 + 2 * qd) * 2048 + ch * 1024, 121 * 2048 + 1024);
    l.r2 = mks(w2, rb * 24 * 2048 + ch * 1024, 23 * 2048 + 1024);
    l.rw = mks(ws, 0, WS_USED);
    l.gq = QP_OFF + ((hkv * 8 + (lane >> 5)) * 2 + (xcd & 1)) * 4096 + (lane & 31) * 16;
    l.go = PO_OFF + ((ho >> 3) * 64 + (lane >> 3)) * 4096 + qd * 1024 + (ho & 7) * 128 + ((lane & 7) ^ g) * 16;
    l.go1 = l.go ^ 64;
    l.gm = MLF ? ML_OFF + ((ho >> 3) * 64 + lane) * 128 + (ho & 7) * 16 : ML_OFF + ho * 1024 + lane * 16;
    l.gh0 = hq(6 * rb + g);
    l.gh1 = hq(6 * rb + 4 + g) | (g < 2 ? 0u : OOB);
    l.gx = hq(768 + 8 * ex + 2 * g) | (has1 ? OOB : 0u);
    if (lane == 0) lput<u32>(L_PG, 0);
    lbar();
    STAMP(1);
    lrun<4, E1>(l, lane, stamp);
    const u32 tag = lget<u32>(L_TG);
    {
      const rsrc_t rw = mk(ws, 0, WS_USED);
      const u32 ad[4] = {l.gh0, l.gh1, l.gx, l.gx + 16};
      u32x4 h[4];
#pragma unroll
      for (int i = 0; i < 4; ++i) h[i] = lget<u32x4>(L_GH + i * 1024 + l.vv);
      for (u32 spin = 0; spin < SPIN_CAP; ++spin) {
        bool b[4];
#pragma unroll
        for (int i = 0; i < 4; ++i) b[i] = (df4(h[i], tag) & 0xFFFu) != 0 && ad[i] < OOB;
        if (__builtin_amdgcn_ballot_w64((b[0] | b[1]) | (b[2] | b[3])) == 0) break;
        asm volatile("" ::: "memory");
#pragma unroll
        for (int i = 0; i < 4; ++i) if (b[i]) h[i] = ldc(rw, ad[i], 0);
#pragma unroll
        for (int i = 0; i < 4; ++i) if (b[i]) lput<u32x4>(L_GH + i * 1024 + l.vv, h[i]);
      }
      asm volatile("" ::: "memory");
      if (lane == 0) lput<u32>(L_PG, NLOAD + 1);
    }
    __builtin_amdgcn_s_setprio(0);
    {
      const u32 xf = (u32)lget<unsigned short>(L_XF + (n & 1) * 16 + (p & 1) * 8 + g * 2) << 16;
      u32 t0, cn;
      u64 s1, s7;
      asm volatile(FIN
                   : [t0] "=&s"(t0), [cn] "=&s"(cn), [s1] "=&s"(s1), [s7] "=&s"(s7)
                   : [va] "v"(SP_OFF + (lane >> 1) * 8192 + p * 32 + (lane & 1) * 16), [oa] "v"((n & 1) * 2048 + p * 8 + g * 2), [xf] "v"(xf),
                     [tg] "v"(tag), [ea] "v"(p + lane == 0 ? 0u : OOB), [fs1] "n"(FS1), [fd1] "n"(FD1), [rs] "s"(l.rw), [out] "s"(out), [ls] "n"(L_S), [fs] "n"(FS), [fd] "n"(FD)
                   : "memory", "vcc", "scc", FIN_CLOB);
      STAMP(14);
    }
    asm volatile(".Lcode1:");
  } else {
  u32x4 WQ[8];
  const rsrc_t rx = mk(x, 0, 4096);
  const u32x4 xs = __builtin_amdgcn_raw_buffer_load_b128(rx, vxs, 0, 0);
  const u32x4 xa = __builtin_amdgcn_raw_buffer_load_b128(rx, vxa, sxa, 0);
  SB();
  {
    const rsrc_t r = mk(wq, sq, 32 * 8192 - 7936);
#pragma unroll
    for (int j = 0; j < 8; ++j) WQ[j] = ldw(r, vq, j * 8192);
  }
  SB();
  const rsrc_t rw = mk(ws, 0, WS_USED);
  const u32 epoch = *(const __attribute__((address_space(4))) u32*)ws;
  SB();
  STAMP(1);
  if (lane == 0) { lput<u32>(L_F + w * 4, 0); lput<u32>(L_F2 + w * 4, 0); }
  u32 a_qst = (QP_OFF + (((jj ^ QX) * 2 + (w >> 1)) * 8 + xcd) * 512 + (w & 1) * 256 + n * 16) | (lane < 16 ? 0u : OOB);
  u32 l_rq = L_RQ + w * 256 + n * 16, l_fl = lane == 0 ? L_F + w * 4 : L_DM + lane * 4;
  u32x4 mk4;
#pragma unroll
  for (u32 i = 0; i < 4; ++i) mk4[i] = (i == ((n & 7) >> 1)) ? ((n & 1) ? 0xFFFF0000u : 0x0000FFFFu) : 0u;
  u32 l_fa = L_F + (lane & 3) * 4, l_rw = L_RQ + vxs, st1 = 1;
  keep(a_qst); keep(l_rq); keep(l_fl); keep4(mk4); keep(l_fa); keep(l_rw); keep(st1);
  SB();

  u32x4 AX[8];
  {
    pin(xs);
    const float pv = asf(__builtin_amdgcn_readfirstlane(xs[0]) << 16);
    float d[8];
#pragma unroll
    for (int k = 0; k < 4; ++k) { d[2 * k] = blo(xs[k]) - pv; d[2 * k + 1] = bhi(xs[k]) - pv; }
    const float s1 = ((d[0] + d[1]) + (d[2] + d[3])) + ((d[4] + d[5]) + (d[6] + d[7]));
    const float s2 = (d[0] * d[0] + d[1] * d[1] + d[2] * d[2] + d[3] * d[3]) + (d[4] * d[4] + d[5] * d[5] + d[6] * d[6] + d[7] * d[7]);
    float t = tr32(s1, s2);
    t = tr16(t, t);
    t += dpp<XOR1>(t); t += dpp<XOR2>(t); t += dpp<ROR4>(t); t += dpp<ROR8>(t);
    if ((lane & 31) == 0) lput<f32x2>(L_ST + w * 16 + (lane >> 5) * 8, f32x2{t, pv});
    lbar();
    const f32x4 a = lget<f32x4>(L_ST + (n >> 3) * 32), b = lget<f32x4>(L_ST + (n >> 3) * 32 + 16);
    const float da = a[0] * (1.0f / 512.0f), db = b[0] * (1.0f / 512.0f);
    const float ma = a[1] + da, mb = b[1] + db, dm = ma - mb;
    const float m2 = (a[2] - a[0] * da) + (b[2] - b[0] * db) + dm * dm * 256.0f;
    const u32 mv = cvt2((ma + mb) * 0.5f, fmaxf(m2, 0.0f) * (1.0f / 1024.0f));
    const float mr = blo(mv);
    const float rd = __builtin_amdgcn_rcpf(rbf(__builtin_amdgcn_sqrtf(rbf(bhi(mv) + 1e-5f))));
#pragma unroll
    for (int k = 0; k < 4; ++k) {
      const u32 c = cvt2(blo(xa[k]) - mr, bhi(xa[k]) - mr);
      const float e0 = blo(c) * rd, e1 = bhi(c) * rd;
      AX[2 * k] = onehot(cvt2(e0, e0), mk4);
      AX[2 * k + 1] = onehot(cvt2(e1, e1), mk4);
    }
#pragma unroll
    for (int j = 0; j < 8; ++j) keep4(AX[j]);
  }
  STAMP(2);
  u32 tag = (epoch + 1u) & 0xFFFu;
  keep(tag);
  {
    f32x4 acc = {0, 0, 0, 0};
#pragma unroll
    for (int j = 0; j < 8; ++j) {
      pin(WQ[j]);
      acc = mfma(AX[j], WQ[j], acc);
      SB();
    }
    STAMP(3);
    lput<f32x4>(l_rw, acc);
    u32x4 v[4];
    sync4<0, 1024, 2048, 3072>(st1, l_fl, l_fa, l_rq, l_rq, v);
    stc4(rw, a_qst, pk4((asf4(v[0]) + asf4(v[1])) + (asf4(v[2]) + asf4(v[3])), tag));
  }
  SB();
  STAMP(4);
  if (tid == 0) lput<u32>(L_TG, tag);
#pragma unroll
  for (int j = 0; j < 4; ++j) lput<u32x4>(L_Z + vxs + j * 4096, u32x4{~tag, ~tag, ~tag, ~tag});
  if ((tid & 127) == (p >> 1)) lput<u32x4>(L_XF + (tid >> 7) * 16, xs);
  u32 a_qld = QP_OFF + ((hkv * 8 + 2 * w + (lane >> 5)) * 2 + (xcd & 1)) * 4096 + (lane & 31) * 16;
  u32 a_post = PO_OFF + p * 4096 + w * 1024 + (n & 7) * 128 + (n >> 3) * 64 + g * 16;
  u32 a_mlst = (MLF ? ML_OFF + p * 128 + lane * 16 : ML_OFF + (hkv * 8 + lane) * 1024 + (p & 63) * 16) | (tid < 8 ? 0u : OOB);
  u32 a_hst = (HP_OFF + (jj * 8 + w * 2 + (n >> 3)) * 1024 + xcd * 128 + (n & 7) * 16) | (lane < 16 ? 0u : OOB);
  u32 a_pold = PO_OFF + ((ho >> 3) * 64 + 32 * (w >> 1) + n) * 4096 + qd * 1024 + (ho & 7) * 128 + (w & 1) * 64 + g * 16;
  const u32 mli = (w >> 1) * 32 + n;
  u32 l_go = L_GO + (w >> 1) * 4096 + n * 128 + (((w & 1) * 4 + g) ^ (n >> 1)) * 16, l_gm = L_GM + mli * 16;
  u32 l_h0 = L_GH + (lane >> 3) * 128 + (n & 1) * 64 + ((n >> 1) & 3) * 4;
  u32 l_h1 = L_GH + 1024 + (g >> 1) * 256 + (n >> 3) * 128 + (n & 3) * 32 + (g & 1) * 8 + (n & 4);
  u32 l_w2 = L_W2 + w * 6144 + lane * 16;
  u32 a_mlld = MLF ? ML_OFF + ((ho >> 3) * 64 + mli) * 128 + (ho & 7) * 16 : ML_OFF + ho * 1024 + mli * 16;
  const u32 cpart = (w * 32 + 2 * n + (g & 1)) * 32 + (g >> 1) * 16;
  u32 a_sst = SP_OFF + p * 4096 + cpart;
  auto hb = [&](u32 Q) { return HP_OFF + (Q >> 2) * 1024 + (n >> 3) * 64 + (Q & 3) * 16; };
  const u32 ht = (n & 1) * 512 + ((n >> 1) & 3) * 4;
  u32 a_h0 = hb(6 * rb + g) + ht, a_h1 = hb(6 * rb + 4 + (g >> 1)) + (n & 3) * 256 + (g & 1) * 8 + (n & 4), a_hx = hb(768 + 8 * ex + 2 * g) + ht;
  u32 l_rh = L_1 + ((2 * ((n >> 2) & 1) + (n & 1)) * 16 + 4 * w + (n >> 3) * 2 + ((n >> 1) & 1)) * 16;
  u32 l_t = w * 8192 + lane * 16;
  u32 l_qw = L_Q + (2 * w + (lane >> 5)) * 272 + (w >> 1) * 64 + n * 16 + (g & 1) * 8;
  u32 l_qr = L_Q + (n & 7) * 272 + ((n >> 2) & 1) * 64 + g * 16 + (n >> 3) * 64;
  u32 l_qs = L_Q + (n & 7) * 272 + ((n >> 2) & 1) * 64 + g * 16 + 64 - (n >> 3) * 64;
  u32 l_pw = L_PX + (w >> 1) * 128 + g * 32 + (n >> 2) * 8 + (w & 1) * 4;
  u32 l_pr = L_PX + (n >> 3) * 128 + g * 32;
  u32x4 sel;
  sel = (n & 8) ? u32x4{0x05040c0cu, 0x07060c0cu, 0x01000c0cu, 0x03020c0cu} : u32x4{0x0c0c0100u, 0x0c0c0302u, 0x0c0c0504u, 0x0c0c0706u};
  keep(a_qld); keep(a_post); keep(a_mlst); keep(a_hst); keep(a_pold); keep(a_mlld);
  keep(a_sst); keep(a_h0); keep(a_h1); keep(a_hx); keep(l_t); keep4(sel);
  keep(l_rh); keep(l_qw); keep(l_qr); keep(l_qs); keep(l_pw); keep(l_pr);
  u32 l_g = L_QG + l_t, l_1 = L_1 + l_t, l_ov = L_QG + vxs, l_mr = L_ML + (n & 7) * 32, l_mw = lane < 8 ? L_ML + lane * 32 + w * 8 : L_DM + lane * 8;
  u32 st2 = 2, st3 = 3, st4 = 4;
  keep(l_g); keep(l_1); keep(l_ov); keep(l_mr); keep(l_mw); keep(st2); keep(st3); keep(st4);
  keep(l_go); keep(l_gm); keep(l_h0); keep(l_h1); keep(l_w2);
  SB();

  u32x4 K[8], V[8];
  {
    waitp<P_K>(l_fa);
#pragma unroll
    for (int j = 0; j < 8; ++j) K[j] = lget<u32x4>(L_K + l_t + j * 1024);
    asm volatile("" ::: "memory");
    lput<u32>(l_fl + (L_F2 - L_F), 1);
    SB();
    waitp<G_Q>(l_fa);
    u32x4 U[8];
#pragma unroll
    for (int j = 0; j < 8; ++j) U[j] = lget<u32x4>(L_V + l_t + j * 1024);
    asm volatile("" ::: "memory");
    lput<u32>(l_fl + (L_F2 - L_F), 3);
#pragma unroll
    for (int j = 0; j < 8; ++j) { V[j] = u32x4{U[j >> 2][j & 3], U[2 + (j >> 2)][j & 3], U[4 + (j >> 2)][j & 3], U[6 + (j >> 2)][j & 3]}; keep4(V[j]); }
    SB();
    waitp<P_Q>(l_fa);
    STAMP(5);
    u32x4 Q[8];
#pragma unroll
    for (int i = 0; i < 8; ++i) Q[i] = lget<u32x4>(l_g + i * 1024);
    SB();
    f32x4 q;
    auto qred = [&]() __attribute__((always_inline)) -> u32 {
      u32x4 e = xt(Q[0], tag);
      u32 bad = or4(e);
      q = asf4(e);
#pragma unroll
      for (int i = 1; i < 8; ++i) { e = xt(Q[i], tag); bad |= or4(e); q += asf4(e); }
      asm volatile("" : "+v"(q));
      return bad;
    };
    const u32 bad0 = qred();
    STAMP(6);
    if (UNLIKELY(any_bad(bad0))) {
      u32 spin = 0, bad;
      do {
        asm volatile("" ::: "memory");
#pragma unroll
        for (int i = 0; i < 8; ++i) again(Q[i], rw, a_qld + i * 512, 0, tag);
        bad = qred();
      } while (any_bad(bad) && ++spin < SPIN_CAP);
    }
    lput<u32x2>(l_qw, u32x2{cvt2(q[0], q[1]), cvt2(q[2], q[3])});
  }
  u32x4 bq[4];
  sync4<0, 0, 128, 128>(st2, l_fl, l_fa, l_qr, l_qs, bq);
  const u32x4 b00 = bq[0], b01 = bq[1], b10 = bq[2], b11 = bq[3];
  STAMP(7);
  {
    const f32x4 z = {0, 0, 0, 0};
    f32x4 c0 = mfma(K[0], b00, z), c1 = mfma(K[1], b01, z), c2 = mfma(K[2], b00, z), c3 = mfma(K[3], b01, z);
    c0 = mfma(K[4], b10, c0); c1 = mfma(K[5], b11, c1); c2 = mfma(K[6], b10, c2); c3 = mfma(K[7], b11, c3);
    const bool same = (((lane >> 2) ^ lane) & 8) == 0;
    float sc[4];
#pragma unroll
    for (int r = 0; r < 4; ++r) {
      auto s = __builtin_amdgcn_permlane32_swap(asu(same ? c0[r] : c1[r]), asu(same ? c2[r] : c3[r]), false, false);
      sc[r] = asf(s[0]) + dpp<ROR8>(asf(s[1]));
    }
#pragma unroll
    for (int r = 0; r < 4; r += 2) {
      const u32 c = cvt2(sc[r], sc[r + 1]);
      const u32 e = cvt2(blo(c) * 0.08838834764831845f, bhi(c) * 0.08838834764831845f);
      sc[r] = blo(e); sc[r + 1] = bhi(e);
    }
    const float m = maxh(max4(sc[0], sc[1], sc[2], sc[3]));
    const float mc = m * LOG2E;
    float e[4];
#pragma unroll
    for (int r = 0; r < 4; ++r) e[r] = ex2(__builtin_fmaf(sc[r], LOG2E, -mc));
    const u32 w01 = cvt2(e[0], e[1]), w23 = cvt2(e[2], e[3]);
    const u32 o01 = asu(dpp<ROR8>(asf(w01))), o23 = asu(dpp<ROR8>(asf(w23)));
    const u32x4 P0 = {__builtin_amdgcn_perm(o01, w01, sel[0]), __builtin_amdgcn_perm(o01, w01, sel[1]),
                      __builtin_amdgcn_perm(o23, w23, sel[0]), __builtin_amdgcn_perm(o23, w23, sel[1])};
    const u32x4 P1 = {__builtin_amdgcn_perm(o01, w01, sel[2]), __builtin_amdgcn_perm(o01, w01, sel[3]),
                      __builtin_amdgcn_perm(o23, w23, sel[2]), __builtin_amdgcn_perm(o23, w23, sel[3])};
    f32x4 o[4];
#pragma unroll
    for (int i = 0; i < 4; ++i) o[i] = mfma(V[i], P0, z);
#pragma unroll
    for (int i = 0; i < 4; ++i) o[i] = mfma(V[4 + i], P1, o[i]);
    SB();
    float l = (e[0] + e[1]) + (e[2] + e[3]);
    l += dpp<ROR8>(l);
    l = tr16(l, l);
    l = tr32(l, l);
    lput<f32x2>(l_mw, f32x2{m, l});
    SB();
#pragma unroll
    for (int i = 0; i < 4; ++i) lput<f32x4>(l_g + i * 1024, o[i]);
  }
  {
    u32x4 r[6];
    sync6(st3, l_fl, l_fa, l_mr, l_ov, r);
    const float m4[4] = {asf(r[0][0]), asf(r[0][2]), asf(r[1][0]), asf(r[1][2])}, l4[4] = {asf(r[0][1]), asf(r[0][3]), asf(r[1][1]), asf(r[1][3])};
    const float M = max4(m4[0], m4[1], m4[2], m4[3]), Mc = M * LOG2E;
    float f[4];
#pragma unroll
    for (int i = 0; i < 4; ++i) f[i] = ex2(__builtin_fmaf(m4[i], LOG2E, -Mc));
    stc4(rw, a_post, pk4(f[0] * asf4(r[2]) + f[1] * asf4(r[3]) + f[2] * asf4(r[4]) + f[3] * asf4(r[5]), tag));
    SB();
    const float L = f[0] * l4[0] + f[1] * l4[1] + f[2] * l4[2] + f[3] * l4[3];
    stc4(rw, a_mlst, u32x4{asu(M) | tag, pk(L, tag), tag, tag});
  }
  SB();
  STAMP(8);

  if (has1) {
    waitp<P_1>(l_fa);
    STAMP(9);
    f32x4 acc = {0, 0, 0, 0}, acd = {0, 0, 0, 0};
    u32x4 T1[8];
#pragma unroll
    for (int j = 0; j < 8; ++j) T1[j] = lget<u32x4>(l_1 + j * 1024);
    SB();
#pragma unroll
    for (int j = 0; j < 8; j += 2) { acc = mfma(AX[j], T1[j], acc); acd = mfma(AX[j + 1], T1[j + 1], acd); }
    lput<f32x4>(l_1, acc + acd);
    u32x4 v[4];
    sync4<0, 8192, 16384, 24576>(st4, l_fl, l_fa, l_rh, l_rh, v);
    stc4(rw, a_hst, pk4((asf4(v[0]) + asf4(v[1])) + (asf4(v[2]) + asf4(v[3])), tag));
  }
  SB();
  STAMP(10);
  {
    const u32x4 rs = mks(ws, 0, WS_USED);
    u32 t0, t1, cn;
    u64 s11, s12, s13;
    asm volatile(TAIL
                 : [t0] "=&s"(t0), [t1] "=&s"(t1), [cn] "=&s"(cn), [s11] "=&s"(s11), [s12] "=&s"(s12), [s13] "=&s"(s13)
                 : [lt] "v"(l_t), [gm] "v"(l_gm), [go] "v"(l_go), [pw] "v"(l_pw), [pr] "v"(l_pr), [fl] "v"(l_fl), [h0] "v"(l_h0), [h1] "v"(l_h1),
                   [w2] "v"(l_w2), [aml] "v"(a_mlld), [apo] "v"(a_pold), [ah0] "v"(a_h0), [ah1] "v"(a_h1), [ahx] "v"(a_hx), [ast] "v"(a_sst),
                   [tg] "v"(tag), [k0] "v"(mk4[0]), [k1] "v"(mk4[1]), [k2] "v"(mk4[2]), [k3] "v"(mk4[3]), [rs] "s"(rs), [jj] "s"(jj),
                   [ls] "n"(L_S), [mo] "n"(MLF ? 2048 : 256), [pwo] "n"(P_WO), [pw2] "n"(P_W2), [po] "n"(SQ.po[0]), [ph] "n"(NLOAD + 1)
                 : "memory", "vcc", "scc", TAIL_CLOB);
    STAMP(14);
  }
  }
}

extern "C" void kernel_launch(void* const* d_in, const int* in_sizes, int n_in, void* d_out, int out_size, void* d_ws,
                              size_t ws_size, hipStream_t stream) {
  (void)in_sizes; (void)n_in; (void)out_size; (void)ws_size;
  (void)hipFuncSetAttribute((const void*)fused_kernel, hipFuncAttributeMaxDynamicSharedMemorySize, LDS_BYTES);
  fused_kernel<<<256, 320, LDS_BYTES, stream>>>(d_in[0], d_in[3], d_in[1], d_ws, d_in[2], d_in[6], d_in[4], d_in[7], (u32*)d_out);
}
```
